```python
import torch

def _softmax(x, axis=-1):
    return torch.softmax(x, dim=axis)
def _gelu(x):
    return torch.nn.functional.gelu(x, approximate="tanh")
def _silu(x):
    return torch.nn.functional.silu(x)
def _sigmoid(x):
    return torch.sigmoid(x)
def _layernorm(x, axis=-1, eps=1e-5):
    m = x.mean(dim=axis, keepdim=True)
    v = x.var(dim=axis, keepdim=True, unbiased=False)
    return (x - m) / torch.sqrt(v + eps)
def _scatter_add(tgt, idx, src, axis=0):
    return tgt.clone().index_add_(axis, idx, src)
def _segment_sum(src, seg, n):
    out = torch.zeros((n,) + src.shape[1:], dtype=src.dtype)
    return out.index_add_(0, seg, src)
def _pad(x, pw):
    flat = tuple(v for pair in reversed(pw) for v in pair)
    return torch.nn.functional.pad(x, flat)

kernel_name = 'interleave_layers__sliding_block_cached__dense_activation_silu__decode'

def setup_inputs():

    _w = (lambda a: (lambda t: t.to(torch.bfloat16) if t.is_floating_point() else t)(torch.as_tensor(a)))
    def _A_setup():
        B, Sq, D = 1, 1, 448
        Hq, Hkv, Sk, Dh = 16, 8, 65536, 256
        assert Hq % Hkv == 0, (Hq, Hkv)
        return {
            "x": _w((lambda *s: torch.randn(*s, dtype=torch.bfloat16) * 0.05)(B, Sq, D)),
            "k_cache": _w((lambda *s: torch.randn(*s, dtype=torch.bfloat16) * 0.8)(B, Hkv, Sk, Dh)),
            "v_cache": _w((lambda *s: torch.randn(*s, dtype=torch.bfloat16) * 1.6)(B, Hkv, Sk, Dh)),
            "wq": _w((lambda *s: torch.randn(*s, dtype=torch.bfloat16) * 0.8)(D, Hq * Dh) * (D ** -0.5)),
            "wo": _w((lambda *s: torch.randn(*s, dtype=torch.bfloat16) * 1.6)(Hq * Dh, D) * ((Hq * Dh) ** -0.5)),
        }
    _A_in = _A_setup()

    def _B_setup():
        B, S, D, F = 1, 1, 448, 1792
        return {
            "x": _w((lambda *s: torch.randn(*s, dtype=torch.bfloat16) * 0.05)(B, S, D)),
            "w1": _w((lambda *s: torch.randn(*s, dtype=torch.bfloat16) * 0.4)(D, F) * (D ** -0.5)),
            "w2": _w((lambda *s: torch.randn(*s, dtype=torch.bfloat16) * 0.4)(F, D) * (F ** -0.5)),
        }
    _B_in = _B_setup()

    return {
        "A_x": _A_in["x"],
        "A_k_cache": _A_in["k_cache"],
        "A_v_cache": _A_in["v_cache"],
        "A_wq": _A_in["wq"],
        "A_wo": _A_in["wo"],
        "B_x": _B_in["x"],
        "B_w1": _B_in["w1"],
        "B_w2": _B_in["w2"],
    }

def reference(A_x, A_k_cache, A_v_cache, A_wq, A_wo, B_x, B_w1, B_w2):
    def _A_ref(x, k_cache, v_cache, wq, wo):
        B, Sq, D = x.shape
        Hq, Dh = 16, 256
        Hkv, Sk = k_cache.shape[1], k_cache.shape[2]
        W = min(4096, max(256, Sk // 4))
        rep = Hq // Hkv
        q = (lambda x, *d: x.permute(*d))((x @ wq).reshape(B, Sq, Hq, Dh), 0, 2, 1, 3)
        k = (lambda x, n, axis: torch.repeat_interleave(x, n, dim=axis))(k_cache[:, :, Sk - W:, :], rep, axis=1)
        v = (lambda x, n, axis: torch.repeat_interleave(x, n, dim=axis))(v_cache[:, :, Sk - W:, :], rep, axis=1)
        scores = torch.einsum("bhqd,bhkd->bhqk", q, k) * (Dh ** -0.5)
        p = _softmax(scores, axis=-1)
        o = torch.einsum("bhqk,bhkd->bhqd", p, v)
        return (lambda x, *d: x.permute(*d))(o, 0, 2, 1, 3).reshape(B, Sq, Hq * Dh) @ wo

    def _B_ref(x, w1, w2):
        h = torch.einsum("bsd,df->bsf", x, w1)
        h = _silu(h)
        out = torch.einsum("bsf,fd->bsd", h, w2)
        return out

    x = A_x
    x = x + _A_ref(_layernorm(x, axis=-1), A_k_cache, A_v_cache, A_wq, A_wo)
    x = x + _B_ref(_layernorm(x, axis=-1), B_w1, B_w2)
    x = x + _A_ref(_layernorm(x, axis=-1), A_k_cache, A_v_cache, A_wq, A_wo)
    x = x + _B_ref(_layernorm(x, axis=-1), B_w1, B_w2)
    return x
```

```cpp
#include <hip/hip_runtime.h>
#include <hip/hip_bf16.h>
#include <stdint.h>

typedef unsigned short u16;
__device__ __forceinline__ float bf2f(u16 b) { return __uint_as_float(((unsigned)b) << 16); }
__device__ __forceinline__ u16 f2bf(float f) { unsigned u = __float_as_uint(f); u += 0x7fffu + ((u >> 16) & 1u); return (u16)(u >> 16); }
__device__ __forceinline__ float rbf(float f) { return bf2f(f2bf(f)); }

constexpr int D = 448, HQ = 16, HKV = 8, SK = 65536, DH = 256, W = 4096, F = 1792;

__device__ float block_sum(float v, float* red) {
  for (int o = 32; o > 0; o >>= 1) v += __shfl_xor(v, o);
  __syncthreads();
  if ((threadIdx.x & 63) == 0) red[threadIdx.x >> 6] = v;
  __syncthreads();
  float s = 0;
  for (int i = 0; i < (int)(blockDim.x >> 6); ++i) s += red[i];
  return s;
}

__device__ void ln_to_lds(const float* x, float* xn, float* red) {
  float s = 0;
  for (int i = threadIdx.x; i < D; i += blockDim.x) s += x[i];
  float m = block_sum(s, red) / D;
  float v = 0;
  for (int i = threadIdx.x; i < D; i += blockDim.x) { float d = x[i] - m; v += d * d; }
  v = block_sum(v, red) / D;
  float r = 1.0f / sqrtf(v + 1e-5f);
  for (int i = threadIdx.x; i < D; i += blockDim.x) xn[i] = rbf((x[i] - m) * r);
  __syncthreads();
}

__global__ void k_init(const u16* x, float* xf) { int i = threadIdx.x; if (i < D) xf[i] = bf2f(x[i]); }

template <int ACT>
__global__ void k_ln_gemv(const float* x, const u16* w, int N, float* out) {
  __shared__ float xn[D]; __shared__ float red[16]; __shared__ float part[4][64];
  ln_to_lds(x, xn, red);
  int c = blockIdx.x * 64 + (threadIdx.x & 63), g = threadIdx.x >> 6;
  float a = 0;
  for (int r = g; r < D; r += 4) a += xn[r] * bf2f(w[(size_t)r * N + c]);
  part[g][threadIdx.x & 63] = a;
  __syncthreads();
  if (g == 0) {
    float s = part[0][threadIdx.x] + part[1][threadIdx.x] + part[2][threadIdx.x] + part[3][threadIdx.x];
    s = rbf(s);
    if (ACT) s = rbf(s / (1.0f + __expf(-s)));
    out[c] = s;
  }
}

__global__ void k_attn(const float* q, const u16* kc, const u16* vc, float* po, float* pml) {
  __shared__ float qs[2][DH]; __shared__ float sc[2][128]; __shared__ float red[16];
  int h = blockIdx.x >> 5, j = blockIdx.x & 31, t = threadIdx.x;
  qs[0][t] = q[(2 * h) * DH + t]; qs[1][t] = q[(2 * h + 1) * DH + t];
  __syncthreads();
  const u16* kb = kc + ((size_t)h * SK + (SK - W) + j * 128) * DH;
  const u16* vb = vc + ((size_t)h * SK + (SK - W) + j * 128) * DH;
  { int key = t & 127, hh = t >> 7; float a = 0;
    for (int d = 0; d < DH; ++d) a += qs[hh][d] * bf2f(kb[(size_t)key * DH + d]);
    sc[hh][key] = rbf(rbf(a) * 0.0625f); }
  __syncthreads();
  for (int hh = 0; hh < 2; ++hh) {
    float m = -INFINITY; for (int k = 0; k < 128; ++k) m = fmaxf(m, sc[hh][k]);
    float l = 0, o = 0;
    for (int k = 0; k < 128; ++k) { float p = __expf(sc[hh][k] - m); l += p; o += p * bf2f(vb[(size_t)k * DH + t]); }
    po[((2 * h + hh) * 32 + j) * DH + t] = o;
    if (t == 0) { pml[((2 * h + hh) * 32 + j) * 2] = m; pml[((2 * h + hh) * 32 + j) * 2 + 1] = l; }
  }
}
__global__ void k_comb(const float* po, const float* pml, float* o) {
  int hq = blockIdx.x, t = threadIdx.x;
  float M = -INFINITY; for (int j = 0; j < 32; ++j) M = fmaxf(M, pml[(hq * 32 + j) * 2]);
  float L = 0, a = 0;
  for (int j = 0; j < 32; ++j) { float w = __expf(pml[(hq * 32 + j) * 2] - M); L += w * pml[(hq * 32 + j) * 2 + 1]; a += w * po[(hq * 32 + j) * DH + t]; }
  o[hq * DH + t] = rbf(a / L);
}
__global__ void k_gemv_res(const float* in, const u16* w, int R, float* x, u16* out) {
  __shared__ float part[16][64];
  int c = blockIdx.x * 64 + (threadIdx.x & 63), g = threadIdx.x >> 6;
  float a = 0;
  for (int r = g; r < R; r += 16) a += in[r] * bf2f(w[(size_t)r * D + c]);
  part[g][threadIdx.x & 63] = a;
  __syncthreads();
  if (g == 0) {
    float s = 0; for (int i = 0; i < 16; ++i) s += part[i][threadIdx.x];
    float y = rbf(x[c] + rbf(s));
    x[c] = y; if (out) out[c] = f2bf(y);
  }
}

extern "C" void kernel_launch(void* const* d_in, const int* in_sizes, int n_in,
                              void* d_out, int out_size, void* d_ws, size_t ws_size,
                              hipStream_t stream) {
  const u16* x = (const u16*)d_in[0]; const u16* kc = (const u16*)d_in[1]; const u16* vc = (const u16*)d_in[2];
  const u16* wq = (const u16*)d_in[3]; const u16* wo = (const u16*)d_in[4];
  const u16* w1 = (const u16*)d_in[6]; const u16* w2 = (const u16*)d_in[7];
  float* ws = (float*)d_ws;
  float* xf = ws; float* q = ws + 512; float* o = q + 4096; float* h = o + 4096; float* pml = h + 2048; float* po = pml + 1024;
  k_init<<<1, 512, 0, stream>>>(x, xf);
  for (int l = 0; l < 2; ++l) {
    k_ln_gemv<0><<<64, 256, 0, stream>>>(xf, wq, HQ * DH, q);
    k_attn<<<256, 256, 0, stream>>>(q, kc, vc, po, pml);
    k_comb<<<16, 256, 0, stream>>>(po, pml, o);
    k_gemv_res<<<7, 1024, 0, stream>>>(o, wo, HQ * DH, xf, nullptr);
    k_ln_gemv<1><<<28, 256, 0, stream>>>(xf, w1, F, h);
    k_gemv_res<<<7, 1024, 0, stream>>>(h, w2, F, xf, l == 1 ? (u16*)d_out : nullptr);
  }
}
```

```cpp
#include <hip/hip_runtime.h>
#include <stdint.h>

#define STAMPS 0

namespace {

typedef unsigned v4u __attribute__((ext_vector_type(4)));
typedef unsigned v2u __attribute__((ext_vector_type(2)));
typedef short s4 __attribute__((ext_vector_type(4)));
typedef __bf16 bf8 __attribute__((ext_vector_type(8)));
typedef __bf16 bf2 __attribute__((ext_vector_type(2)));
typedef float f4 __attribute__((ext_vector_type(4)));
typedef float f2 __attribute__((ext_vector_type(2)));
typedef __amdgpu_buffer_rsrc_t rsrc_t;
#define LAS __attribute__((address_space(3)))
#define DEV __device__ __forceinline__

constexpr int DM = 448, FF = 1792, SK = 65536, WIN = 4096;
constexpr int NA = 28;
constexpr unsigned EMPTY = 0xFFFFFFFFu;
constexpr int OOB = 0x40000000;
constexpr int SC1 = 16;
constexpr unsigned SPIN_CAP = 40000;

constexpr int OFF_OB = 0;
constexpr int OFF_ML = 262144;
constexpr int OFF_PC = OFF_ML + 16384;
constexpr int OFF_PD = OFF_PC + 16384;
constexpr int OFF_Q2 = OFF_PD + 32768;
constexpr int OFF_HB = OFF_Q2 + 16384;
constexpr int OFF_HC = OFF_HB + 16384;
constexpr int LSTR = OFF_HC + 8192;
constexpr int OFF_QT = 2 * LSTR;
constexpr int OFF_E = OFF_QT + 32768;
constexpr int ARENA = 1 << 21;
constexpr __host__ __device__ int phys(int o) { return ((o >> 13) << 14) | (o & 8191); }
constexpr int PLSTR = 2 * LSTR;
static_assert(LSTR % 8192 == 0 && phys(OFF_E) + 8192 < ARENA, "arena");

constexpr int L_K = 0;
constexpr int L_V = 65536;
constexpr int L_WQ = L_V + 148 * 512;
constexpr int L_W1 = 0, L_W2 = 57344;
constexpr int L_X = L_WQ + 14336;
constexpr int L_Q = L_X + 1024;
constexpr int L_S = L_Q + 1152;
constexpr int L_SX = L_S + 1024;
constexpr int L_O = L_SX + 512;
constexpr int L_H = L_O + 512;
constexpr int L_M = L_H + 128;
constexpr int L_P = 160256;
constexpr int L_Z = L_P + 3072 + 128;
static_assert(L_M + 64 <= L_P && L_P % 256 == 0 && L_Z + 384 <= 163840, "lds");

#define T_TAG 300
#define T_MLP 300
#define T_C0 760
#define DLY_A 10
#define DLY_B0 9
#define DLY_B1 18
#define DLY_C1 60
#define DLY_D 36
#define DLY_DM 10

struct Params {
  const unsigned char* x; const unsigned char* kc; const unsigned char* vc;
  const unsigned char* wq; const unsigned char* wo; const unsigned char* w1; const unsigned char* w2;
  unsigned char* out; unsigned char* arena;
};

DEV f4 mfma(v4u a, v4u b, f4 c) { return __builtin_amdgcn_mfma_f32_16x16x32_bf16(__builtin_bit_cast(bf8, a), __builtin_bit_cast(bf8, b), c, 0, 0, 0); }
DEV unsigned cvtpk(float lo, float hi) { return __builtin_bit_cast(unsigned, __builtin_convertvector(f2{lo, hi}, bf2)); }
DEV float dot2(unsigned a, unsigned b, float c) { return __builtin_amdgcn_fdot2_f32_bf16(__builtin_bit_cast(bf2, a), __builtin_bit_cast(bf2, b), c, false); }
template <int C> DEV float dppf(float x) { return __builtin_bit_cast(float, __builtin_amdgcn_mov_dpp(__builtin_bit_cast(int, x), C, 0xf, 0xf, true)); }
template <int C> DEV unsigned dppu(unsigned x) { return (unsigned)__builtin_amdgcn_mov_dpp((int)x, C, 0xf, 0xf, true); }
constexpr int X1 = 0xB1, X2 = 0x4E, HMIR = 0x141, MIR = 0x140, ROR8 = 0x128;
DEV float fmx(float a, float b) { return __builtin_amdgcn_fmed3f(a, b, __builtin_inff()); }
DEV float sum16(float v) { v += dppf<X1>(v); v += dppf<X2>(v); v += dppf<HMIR>(v); v += dppf<MIR>(v); return v; }
DEV float max16(float v) { v = fmaxf(v, dppf<X1>(v)); v = fmaxf(v, dppf<X2>(v)); v = fmaxf(v, dppf<HMIR>(v)); v = fmaxf(v, dppf<MIR>(v)); return v; }
DEV float sum_x16(float v) { auto s = __builtin_amdgcn_permlane16_swap(__float_as_uint(v), __float_as_uint(v), false, false); return __uint_as_float(s[0]) + __uint_as_float(s[1]); }
DEV float sum_x32(float v) { auto s = __builtin_amdgcn_permlane32_swap(__float_as_uint(v), __float_as_uint(v), false, false); return __uint_as_float(s[0]) + __uint_as_float(s[1]); }
DEV float max_x16(float v) { auto s = __builtin_amdgcn_permlane16_swap(__float_as_uint(v), __float_as_uint(v), false, false); return fmaxf(__uint_as_float(s[0]), __uint_as_float(s[1])); }
DEV float max_x32(float v) { auto s = __builtin_amdgcn_permlane32_swap(__float_as_uint(v), __float_as_uint(v), false, false); return fmaxf(__uint_as_float(s[0]), __uint_as_float(s[1])); }

DEV rsrc_t mkrsrc(const void* p, int bytes) { return __builtin_amdgcn_make_buffer_rsrc((void*)p, 0, bytes, 0x00020000); }
DEV v4u ld16(rsrc_t r, int v, int s) { return __builtin_amdgcn_raw_buffer_load_b128(r, v, s, SC1); }
DEV v2u ld8(rsrc_t r, int v, int s) { return __builtin_amdgcn_raw_buffer_load_b64(r, v, s, SC1); }
DEV void st16(v4u d, rsrc_t r, int v, int s) { __builtin_amdgcn_raw_buffer_store_b128(d, r, v, s, SC1); }
DEV void st8(v2u d, rsrc_t r, int v, int s) { __builtin_amdgcn_raw_buffer_store_b64(d, r, v, s, SC1); }
DEV void pub16(v4u d, rsrc_t r, int v, int s) { st16(d, r, v, s); st16(d, r, v, s + 8192); }
DEV void pub8(v2u d, rsrc_t r, int v, int s) { st8(d, r, v, s); st8(d, r, v, s + 8192); }

DEV void dma16(unsigned lds, rsrc_t r, int v, int s) {
  asm volatile("s_mov_b32 m0, %0\n\ts_nop 0\n\tbuffer_load_dwordx4 %1, %2, %3 offen nt lds" ::"s"(lds), "v"(v), "s"(r), "s"(s) : "memory");
}
DEV void dma16c(unsigned lds, rsrc_t r, int v, int s) {
  asm volatile("s_mov_b32 m0, %0\n\ts_nop 0\n\tbuffer_load_dwordx4 %1, %2, %3 offen lds" ::"s"(lds), "v"(v), "s"(r), "s"(s) : "memory");
}
DEV void dma4(unsigned lds, rsrc_t r, int v) {
  asm volatile("s_mov_b32 m0, %0\n\ts_nop 0\n\tbuffer_load_dword %1, %2, 0 offen lds" ::"s"(lds), "v"(v), "s"(r) : "memory");
}
DEV int opq(int x) { asm volatile("" : "+v"(x)); return x; }
DEV void vmdrain() { __builtin_amdgcn_s_waitcnt(0x0F70); }
template <int N> DEV void vmwait() { asm volatile("s_waitcnt vmcnt(%0)" ::"i"(N) : "memory"); }
DEV void bar() { asm volatile("s_waitcnt lgkmcnt(0)\n\ts_barrier" ::: "memory"); }
template <int N> DEV void nap() {
#pragma unroll
  for (int i = 0; i < N / 16; ++i) __builtin_amdgcn_s_sleep(16);
  if (N % 16) __builtin_amdgcn_s_sleep(N % 16);
}
DEV unsigned long long rtc() { return __builtin_amdgcn_s_memrealtime(); }
DEV void wait_rt(unsigned long long t) { while (rtc() < t) __builtin_amdgcn_s_sleep(1); }
DEV unsigned long long clk() { unsigned long long t; asm volatile("s_memtime %0\n\ts_waitcnt lgkmcnt(0)" : "=s"(t)::"memory"); return t; }

typedef LAS unsigned char* lptr;
DEV v4u lr16(lptr L, int a) { return *(LAS v4u*)(L + a); }
DEV v2u lr8(lptr L, int a) { return *(LAS v2u*)(L + a); }
DEV float lr4f(lptr L, int a) { return *(LAS float*)(L + a); }
DEV v2u ltr(lptr L, int a) { return __builtin_bit_cast(v2u, __builtin_amdgcn_ds_read_tr16_b64_v4i16((LAS s4*)(L + a))); }
DEV v4u ltr2(lptr L, int a, int h) { v2u lo = ltr(L, a), hi = ltr(L, a + h); return v4u{lo.x, lo.y, hi.x, hi.y}; }
DEV bool has_empty(v4u r) { return max(max(r.x, r.y), max(r.z, r.w)) == EMPTY; }
DEV float bflo(unsigned u) { return __uint_as_float(u << 16); }
DEV float bfhi(unsigned u) { return __uint_as_float(u & 0xffff0000u); }

#define STAMP(i)
#define STAMP_ARG
#define STAMP_PASS

DEV v4u selmask(int lane) {
  const unsigned h = (lane & 1) ? 0xffff0000u : 0x0000ffffu; const int d = (lane >> 1) & 3;
  return v4u{d == 0 ? h : 0u, d == 1 ? h : 0u, d == 2 ? h : 0u, d == 3 ? h : 0u};
}

template <int NL, int NB>
DEV void sum_rows(rsrc_t ar, const int (&vo)[NL], int so, v4u sel, f4 (&acc)[NB]) {
  v4u r[NB][NL];
#pragma unroll
  for (int b = 0; b < NB; ++b)
#pragma unroll
    for (int i = 0; i < NL; ++i) r[b][i] = ld16(ar, vo[i] + b * 128, so + i * 16384);
  for (unsigned spin = 0;; ++spin) {
    bool bad = false;
#pragma unroll
    for (int b = 0; b < NB; ++b) {
      f4 a = {0.f, 0.f, 0.f, 0.f};
#pragma unroll
      for (int i = 0; i < NL; ++i) a = mfma(sel, r[b][i], a);
      acc[b] = a; bad |= a.x != a.x;
    }
    if (__builtin_expect(!__any(bad), 1)) break;
    bool miss = false;
#pragma unroll
    for (int b = 0; b < NB; ++b)
#pragma unroll
      for (int i = 0; i < NL; ++i) {
        const bool e = has_empty(r[b][i]); miss |= e;
        asm volatile("" ::: "memory");
        if (e) r[b][i] = ld16(ar, vo[i] + b * 128, so + i * 16384);
      }
    vmdrain();
    if (!__any(miss) || spin > SPIN_CAP) break;
  }
#pragma unroll
  for (int b = 0; b < NB; ++b) {
    acc[b].x += dppf<ROR8>(acc[b].x); acc[b].y += dppf<ROR8>(acc[b].y);
    acc[b].z += dppf<ROR8>(acc[b].z); acc[b].w += dppf<ROR8>(acc[b].w);
  }
}

DEV void ln_stats(lptr L, int xl, float& mu, float& rstd) {
  const v4u xs = lr16(L, xl);
  float s1 = dot2(xs.x, 0x3f803f80u, 0.f), s2 = dot2(xs.x, xs.x, 0.f);
  s1 = dot2(xs.y, 0x3f803f80u, s1); s2 = dot2(xs.y, xs.y, s2);
  s1 = dot2(xs.z, 0x3f803f80u, s1); s2 = dot2(xs.z, xs.z, s2);
  s1 = dot2(xs.w, 0x3f803f80u, s1); s2 = dot2(xs.w, xs.w, s2);
  s1 = sum_x32(sum_x16(sum16(s1))); s2 = sum_x32(sum_x16(sum16(s2)));
  mu = s1 * (1.f / DM);
  const float var = fmaxf(fmaf(-mu, mu, s2 * (1.f / DM)), 0.f);
  rstd = __builtin_amdgcn_rsqf(var + 1e-5f);
}

DEV f4 q_gemv(lptr L, int xa, int xl, const v4u (&wf)[14], f4 cs) {
  f4 a = {0.f, 0.f, 0.f, 0.f};
  v4u xf[14];
#pragma unroll
  for (int s = 0; s < 14; ++s) xf[s] = lr16(L, xa + s * 64);
  float mu, rstd; ln_stats(L, xl, mu, rstd);
#pragma unroll
  for (int s = 0; s < 14; ++s) a = mfma(wf[s], xf[s], a);
  return f4{fmaf(-mu, cs.x, a.x) * rstd, fmaf(-mu, cs.y, a.y) * rstd, fmaf(-mu, cs.z, a.z) * rstd, fmaf(-mu, cs.w, a.w) * rstd};
}

DEV void attn_compute(const Params& p, lptr L, unsigned lb, int g, int r, int u, int lane STAMP_ARG) {
  const int start = (1024 * r) / 7, nkeys = (1024 * (r + 1)) / 7 - start;
  const rsrc_t kr = mkrsrc(p.kc + ((size_t)g * SK + (SK - WIN)) * 512, WIN * 512);
  const rsrc_t vr = mkrsrc(p.vc + ((size_t)g * SK + (SK - WIN)) * 512, WIN * 512);
  const rsrc_t ar = mkrsrc(p.arena, ARENA);
  const int hi = lane >> 5, pos = lane & 31, gq = lane >> 4, n = lane & 15;
  {
    const int v0 = hi * 512 + (pos ^ hi) * 16;
#pragma unroll
    for (int k = 0; k < 8; ++k) dma16(lb + L_K + u * 8192 + k * 1024, kr, v0 ^ (k * 32), (start + u * 16 + 2 * k) * 512);
  }
  bar();
  {
    const int v0 = hi * 512 + (pos ^ (hi << 1)) * 16;
#pragma unroll
    for (int k = 0; k < 8; ++k) dma16(lb + L_V + u * 8192 + k * 1024, vr, v0 ^ ((k & 3) * 64), (start + u * 16 + 2 * k) * 512);
  }
  STAMP(0);
  const int e = u >> 1, qa = opq(L_Q + (n & 1) * 576 + gq * 16), qb = opq(qa + (u & 1) * 256);
  const int sw = opq(L_S + (n & 1) * 512 + u * 128 + gq * 16), sxw = opq(L_SX + (u & 1) * 256 + (n & 1) * 128 + e * 64 + gq * 16);
  const int sr = opq(L_S + hi * 512 + pos * 4), sxr = opq(L_SX + hi * 128 + pos * 4);
  const int pw = opq(L_P + u * 768 + hi * 320 + ((pos >> 2) & 3) * 16 + ((pos & 3) + 4 * (pos >> 4)) * 2);
  const int pr = L_P + u * 768 + (n & 1) * 320 + gq * 16;
  const int pr0 = opq((n & 2) ? L_Z : pr), pr1 = opq((n & 2) ? pr : L_Z);
  const int va = L_V + (lane >> 2) * 512 + (((8 * u + ((lane >> 1) & 1)) ^ (((lane >> 2) & 7) << 1)) * 16) + 8 * (lane & 1);
  int vt[4], vu[4], vw[4];
#pragma unroll
  for (int t = 0; t < 4; ++t) { vt[t] = opq(va ^ (t * 32)); vu[t] = opq(vt[t] + 65536); vw[t] = opq(vu[t] + 8192 - ((lane >> 2) & 12) * 512); }
  const int ov = opq(n < 4 ? phys(OFF_OB + ((g * 2 + (n & 1)) * 32 + r) * 512) + (2 * u + (n >> 1)) * 64 + gq * 16 : OOB);
  const int mv = opq(pos == 0 ? phys(OFF_ML + (g * 32 + r) * 64) + hi * 8 : OOB);
  const bool live4 = 128 + pos < nkeys;
  constexpr float CS = 0.0625f * 1.4426950408889634f;
  v4u kf[20], vf[20];
  vmwait<8>();
#pragma unroll 1
  for (int layer = 0; layer < 2; ++layer) {
    bar();
    STAMP(1 + 6 * layer);
    if (layer == 0) {
#pragma unroll
      for (int t = 0; t < 2; ++t)
#pragma unroll
        for (int s = 0; s < 8; ++s) kf[t * 8 + s] = lr16(L, L_K + (2 * u + t) * 8192 + n * 512 + (((4 * s + gq) ^ n) * 16));
      const int xr = e ? 16 + (n & 3) : n;
#pragma unroll
      for (int s = 0; s < 4; ++s) kf[16 + s] = lr16(L, L_WQ + xr * 512 + (((16 * (u & 1) + 4 * s + gq) ^ (xr & 15)) * 16));
    }
    {
      v4u qf[8], qx[4];
#pragma unroll
      for (int s = 0; s < 8; ++s) qf[s] = lr16(L, qa + s * 64);
#pragma unroll
      for (int s = 0; s < 4; ++s) qx[s] = lr16(L, qb + s * 64);
      f4 s0 = {0.f, 0.f, 0.f, 0.f}, s1 = s0, sx = s0;
#pragma unroll
      for (int s = 0; s < 8; ++s) { s0 = mfma(kf[s], qf[s], s0); s1 = mfma(kf[8 + s], qf[s], s1); }
#pragma unroll
      for (int s = 0; s < 4; ++s) sx = mfma(kf[16 + s], qx[s], sx);
      if (n < 2) { *(LAS f4*)(L + sw) = s0; *(LAS f4*)(L + sw + 64) = s1; *(LAS f4*)(L + sxw) = sx; }
    }
    if (layer == 0) vmwait<0>();
    bar();
    STAMP(2 + 6 * layer);
    float t0 = lr4f(L, sr), t1 = lr4f(L, sr + 128), t2 = lr4f(L, sr + 256), t3 = lr4f(L, sr + 384);
    float t4 = lr4f(L, sxr) + lr4f(L, sxr + 256);
    if (layer == 0) {
#pragma unroll
      for (int s = 0; s < 4; ++s)
#pragma unroll
        for (int t = 0; t < 4; ++t) vf[s * 4 + t] = ltr2(L, vt[t] + s * 16384, 8192);
    }
    t4 = live4 ? t4 : -__builtin_inff();
    float m = fmaxf(fmaxf(fmaxf(t0, t1), fmaxf(t2, t3)), t4);
    m = max_x16(max16(m));
    m = __builtin_ceilf(m * CS);
    const float p0 = __builtin_amdgcn_exp2f(fmaf(t0, CS, -m)), p1 = __builtin_amdgcn_exp2f(fmaf(t1, CS, -m)),
                p2 = __builtin_amdgcn_exp2f(fmaf(t2, CS, -m)), p3 = __builtin_amdgcn_exp2f(fmaf(t3, CS, -m)),
                p4 = __builtin_amdgcn_exp2f(fmaf(t4, CS, -m));
    const unsigned b01 = cvtpk(p0, p1), b23 = cvtpk(p2, p3), b4 = cvtpk(p4, p4);
    *(LAS unsigned short*)(L + pw) = (unsigned short)b01;
    *(LAS unsigned short*)(L + pw + 64) = (unsigned short)(b01 >> 16);
    *(LAS unsigned short*)(L + pw + 128) = (unsigned short)b23;
    *(LAS unsigned short*)(L + pw + 192) = (unsigned short)(b23 >> 16);
    *(LAS unsigned short*)(L + pw + 256) = (unsigned short)b4;
    asm volatile("" ::: "memory");
    f4 o0 = {0.f, 0.f, 0.f, 0.f}, o1 = o0;
#pragma unroll
    for (int s = 0; s < 4; ++s) {
      const v4u pa = lr16(L, pr0 + s * 64), pb = lr16(L, pr1 + s * 64);
      o0 = mfma(vf[s * 4 + 0], pa, o0); o1 = mfma(vf[s * 4 + 1], pa, o1);
      o0 = mfma(vf[s * 4 + 2], pb, o0); o1 = mfma(vf[s * 4 + 3], pb, o1);
    }
    const v4u pa = lr16(L, pr0 + 256), pb = lr16(L, pr1 + 256);
    float l = ((bflo(b01) + bfhi(b01)) + (bflo(b23) + bfhi(b23))) + bflo(b4);
    l = sum_x16(sum16(l));
    if (layer == 0) {
      bar();
      STAMP(3);
#pragma unroll
      for (int t = 0; t < 4; ++t) {
        const v2u lo = ltr(L, vu[t]), hh = ltr(L, vw[t]);
        vf[16 + t] = v4u{lo.x, lo.y, hh.x, hh.y};
      }
    }
    o0 = mfma(vf[16], pa, o0); o1 = mfma(vf[17], pa, o1);
    o0 = mfma(vf[18], pb, o0); o1 = mfma(vf[19], pb, o1);
    pub16(v4u{cvtpk(o0.x, o0.y), cvtpk(o0.z, o0.w), cvtpk(o1.x, o1.y), cvtpk(o1.z, o1.w)}, ar, ov, layer * PLSTR);
    if (u == 0) pub8(v2u{__float_as_uint(m), __float_as_uint(l)}, ar, mv, layer * PLSTR);
    STAMP(4 + 6 * layer);
    vmdrain();
    bar();
    if (layer == 0) bar();
  }
}

DEV void mlp_dma(const Params& p, unsigned lb, int m, int w, int lane) {
  const rsrc_t r1 = mkrsrc(p.w1, DM * FF * 2), r2 = mkrsrc(p.w2, DM * FF * 2);
  const int v1 = (lane >> 3) * (FF * 2) + (lane & 7) * 16;
#pragma unroll
  for (int k = 0; k < 7; ++k) dma16(lb + L_W1 + (w * 7 + k) * 1024, r1, v1, (w * 7 + k) * 8 * (FF * 2) + m * 128);
#pragma unroll
  for (int k = 0; k < 7; ++k) dma16(lb + L_W2 + (w * 7 + k) * 1024, r2, lane * 16, m * 57344 + (w * 7 + k) * 1024);
}

DEV void mlp_compute(const Params& p, lptr L, unsigned lb, int m, int u, int lane, unsigned long long t_start STAMP_ARG) {
  const rsrc_t ar = mkrsrc(p.arena, ARENA);
  const int gq = lane >> 4, n = lane & 15;
  bar();
  if (m < NA) { wait_rt(t_start + T_MLP); mlp_dma(p, lb, m, u, lane); }
  vmwait<0>();
  STAMP(0);
  bar();
  if (m >= NA) { bar(); return; }
  v4u w1f[14], w2f[16];
  {
    const int a1 = L_W1 + (lane >> 2) * 128 + u * 32 + (lane & 3) * 8;
#pragma unroll
    for (int s = 0; s < 14; ++s) w1f[s] = ltr2(L, a1 + s * 4096, 2048);
    const int pair0 = u < 2 ? 4 * u : 3 * u + 2;
    const int a2 = opq(L_W2 + (lane >> 2) * 896 + pair0 * 64 + (lane & 3) * 8);
#pragma unroll
    for (int s = 0; s < 2; ++s)
#pragma unroll
      for (int k = 0; k < 8; ++k) w2f[s * 8 + k] = ltr2(L, a2 + s * 28672 + k * 32, 14336);
  }
  float cs;
  {
    const v4u ones = {0x3f803f80u, 0x3f803f80u, 0x3f803f80u, 0x3f803f80u};
    f4 c = {0.f, 0.f, 0.f, 0.f};
#pragma unroll
    for (int s = 0; s < 14; ++s) c = mfma(ones, w1f[s], c);
    cs = c.x;
  }
  const int xa = opq(L_X + gq * 16), xl = opq(L_X + lane * 16);
  const int hw = opq(L_H + ((u >> 1) * 4 + (n >> 2)) * 16 + ((u & 1) * 4 + (n & 3)) * 2);
  int hr[4];
#pragma unroll
  for (int k = 0; k < 4; ++k) hr[k] = opq((n & 3) == k ? L_H + gq * 16 : L_Z);
  const int dv = opq(n < (u < 2 ? 4 : 3) ? phys(OFF_PD + m * 1024) + ((u < 2 ? 4 * u : 3 * u + 2) + n) * 64 + gq * 16 : OOB);
  STAMP(1);
#pragma unroll 1
  for (int layer = 0; layer < 2; ++layer) {
    bar();
    STAMP(2 + 4 * layer);
    {
      v4u xf[14];
#pragma unroll
      for (int s = 0; s < 14; ++s) xf[s] = lr16(L, xa + s * 64);
      float mu, rstd; ln_stats(L, xl, mu, rstd);
      f4 a = {0.f, 0.f, 0.f, 0.f};
#pragma unroll
      for (int s = 0; s < 14; ++s) a = mfma(xf[s], w1f[s], a);
      const float z = bflo(cvtpk(fmaf(-mu, cs, a.x) * rstd, 0.f));
      const float h = z * __builtin_amdgcn_rcpf(1.f + __builtin_amdgcn_exp2f(z * -1.4426950408889634f));
      if (lane < 16) *(LAS unsigned short*)(L + hw) = (unsigned short)cvtpk(h, h);
    }
    bar();
    STAMP(3 + 4 * layer);
    f4 o0 = {0.f, 0.f, 0.f, 0.f}, o1 = o0;
#pragma unroll
    for (int s = 0; s < 2; ++s)
#pragma unroll
      for (int k = 0; k < 4; ++k) {
        const v4u hb = lr16(L, hr[k] + s * 64);
        o0 = mfma(w2f[s * 8 + 2 * k], hb, o0); o1 = mfma(w2f[s * 8 + 2 * k + 1], hb, o1);
      }
    pub16(v4u{cvtpk(o0.x, o0.y), cvtpk(o0.z, o0.w), cvtpk(o1.x, o1.y), cvtpk(o1.z, o1.w)}, ar, dv, layer * PLSTR);
    STAMP(4 + 4 * layer);
    if (layer == 0) bar();
  }
}

template <bool A>
DEV void service(const Params& p, lptr L, unsigned lb, int b, int g, int r, int u, int lane, unsigned long long t_start STAMP_ARG) {
  const rsrc_t ar = mkrsrc(p.arena, ARENA);
  const int hi = lane >> 5, pos = lane & 31, gq = lane >> 4, n = lane & 15;
  const int m = (r - NA) * 8 + g;
  const int start = (1024 * r) / 7;
  const rsrc_t kr = mkrsrc(p.kc + ((size_t)g * SK + (SK - WIN)) * 512, WIN * 512);
  const rsrc_t vr = mkrsrc(p.vc + ((size_t)g * SK + (SK - WIN)) * 512, WIN * 512);
  const int hh = r >= 14, cb = r - 14 * hh;
  const int side = (__builtin_amdgcn_s_getreg(20 | (3 << 11)) >> 2) & 1;
  unsigned long long tag;
  {
    const unsigned char* ep = p.arena + phys(OFF_E);
    asm volatile("s_load_dwordx2 %0, %1, 0x0\n\ts_waitcnt lgkmcnt(0)" : "=s"(tag) : "s"(ep) : "memory");
    tag += 0x9E3779B97F4A7C15ull;
  }
  const unsigned tlo = (unsigned)tag & 0xfffe0000u, thi = (unsigned)(tag >> 32);

  if (u == 3) {
    const rsrc_t xr = mkrsrc(p.x, DM * 2), qr = mkrsrc(p.wq, DM * 8192);
#pragma unroll
    for (int k = 0; k < 4; ++k) {
      const int d = k * 64 + lane, v = d >> 1;
      dma4(lb + L_X + k * 256, xr, ((v >> 3) * 8 + (v & 1) * 4 + ((v >> 1) & 3)) * 8 + (d & 1) * 4);
    }
    const int v0 = (lane >> 1) * 8192 + (lane & 1) * 16;
#pragma unroll
    for (int k = 0; k < 14; ++k) dma16(lb + L_WQ + k * 1024, qr, v0, k * 32 * 8192 + (g * 512 + r * 16) * 2);
  } else if (u == 0) {
    int vx, vy;
    const int q2 = phys(OFF_Q2 + (g * 32 + r) * 64);
    if (A) {
      vx = phys(OFF_OB + ((g * 2 + hi) * 32 + r) * 512) + pos * 16;
      vy = lane == 0 ? phys(OFF_ML + (g * 32 + r) * 64) : lane < 5 ? phys(OFF_PC + (2 * g + hh) * 1024) + cb * 64 + (lane - 1) * 16
         : lane == 5 ? phys(OFF_HB + b * 64) : lane < 8 ? q2 + (lane - 6) * 16 : OOB;
    } else {
      vx = lane < 56 ? (m < NA ? phys(OFF_PD + m * 1024) + lane * 16 : OOB) : lane == 56 ? phys(OFF_HC + m * 64) : lane < 59 ? q2 + (lane - 57) * 16 : OOB;
      vy = (m == 0 && lane < 56) ? phys(OFF_PD + NA * 1024) + lane * 16 : OOB;
    }
    const v4u em = {EMPTY, EMPTY, EMPTY, EMPTY};
    const int c0 = r & 1;
    st16(em, ar, vx, c0 * 8192); st16(em, ar, vx, 8192 - c0 * 8192);
    vmwait<1>(); const unsigned long long ta = clk();
    vmwait<0>(); const unsigned long long tb = clk();
    pub16(em, ar, vy, 0); pub16(em, ar, vx, PLSTR); pub16(em, ar, vy, PLSTR);
    const int nearc = (tb - ta > 160) ? c0 : 1 - c0;
    if (lane == 0) *(LAS unsigned*)(L + L_M) = (unsigned)(nearc ^ side);
    vmdrain();
  } else if (u == 1) {
    if (lane < 24) *(LAS v4u*)(L + L_Z + lane * 16) = v4u{0u, 0u, 0u, 0u};
  }
  if (A) {
    const int v0 = hi * 512 + (pos ^ hi) * 16;
#pragma unroll
    for (int k = 0; k < 8; ++k) dma16(lb + L_K + (4 + u) * 8192 + k * 1024, kr, v0 ^ (k * 32), (start + (4 + u) * 16 + 2 * k) * 512);
  }
  bar();
  const int xa = opq(L_X + gq * 16), xl = opq(L_X + lane * 16);
  v4u wf[16];
  f4 cs = {0.f, 0.f, 0.f, 0.f};
  if (u == 3) {
    const int wa = opq(L_WQ + lane * 8);
    if (A) vmwait<8>(); else vmwait<0>();
    STAMP(0);
#pragma unroll
    for (int s = 0; s < 14; ++s) wf[s] = ltr2(L, wa + s * 1024, 512);
    const v4u ones = {0x3f803f80u, 0x3f803f80u, 0x3f803f80u, 0x3f803f80u};
#pragma unroll
    for (int s = 0; s < 14; ++s) cs = mfma(wf[s], ones, cs);
    const v4u (&wq14)[14] = reinterpret_cast<const v4u (&)[14]>(wf);
    const f4 q = q_gemv(L, xa, xl, wq14, cs);
    const unsigned vote = *(LAS unsigned*)(L + L_M) << 16;
    const unsigned qq = (n & 1) ? cvtpk(q.z, q.w) : cvtpk(q.x, q.y);
    st16(v4u{tlo | vote | (qq & 0xffffu), thi, tlo | vote | (qq >> 16), thi}, ar, n < 2 ? phys(OFF_QT) + phys(b * 128) + gq * 32 + n * 16 : OOB, 0);
    STAMP(1);
    if (A) {
      asm volatile("s_waitcnt lgkmcnt(0)" ::: "memory");
      const int v0 = hi * 512 + (pos ^ hi) * 16;
#pragma unroll
      for (int k = 0; k < 10; ++k) dma16(lb + L_WQ + k * 1024, kr, v0 ^ ((k & 7) * 32), (start + 128 + 2 * k) * 512);
    }
  }
  if (A) {
    const int v0 = hi * 512 + (pos ^ (hi << 1)) * 16;
#pragma unroll
    for (int k = 0; k < 8; ++k) dma16(lb + L_V + (4 + u) * 8192 + k * 1024, vr, v0 ^ ((k & 3) * 64), (start + (4 + u) * 16 + 2 * k) * 512);
    if (u == 3) {
#pragma unroll
      for (int k = 0; k < 10; ++k) dma16(lb + L_V + 65536 + k * 1024, vr, v0 ^ ((k & 3) * 64), (start + 128 + 2 * k) * 512);
    }
  }
  if (u == 0) {
    STAMP(0);
    wait_rt(t_start + T_TAG);
    STAMP(1);
    v4u gq4[4]; v2u ga[4];
    for (unsigned spin = 0;; ++spin) {
      asm volatile("" ::: "memory");
#pragma unroll
      for (int i = 0; i < 4; ++i) ga[i] = ld8(ar, lane * 128, phys(OFF_QT) + i * 16384);
      if (A) {
#pragma unroll
        for (int i = 0; i < 4; ++i) gq4[i] = ld16(ar, (lane >> 3) * 1024 + g * 128 + (lane & 7) * 16, phys(OFF_QT) + i * 16384);
      }
      bool ok = true;
#pragma unroll
      for (int i = 0; i < 4; ++i) {
        ok &= ga[i].y == thi && (ga[i].x & 0xfffe0000u) == tlo;
        if (A) ok &= gq4[i].y == thi && gq4[i].w == thi && (gq4[i].x & 0xfffe0000u) == tlo && (gq4[i].z & 0xfffe0000u) == tlo;
      }
      if (__all(ok) || spin > SPIN_CAP) break;
    }
    int votes = 0;
#pragma unroll
    for (int i = 0; i < 4; ++i) votes += __popcll(__ballot((ga[i].x >> 16) & 1));
    if (lane == 0) *(LAS unsigned*)(L + L_M + 4) = (unsigned)((votes > 128) ^ side);
    if (A) {
#pragma unroll
      for (int i = 0; i < 4; ++i) *(LAS unsigned*)(L + L_Q + (i >> 1) * 576 + (i & 1) * 256 + lane * 4) = (gq4[i].x & 0xffffu) | (gq4[i].z << 16);
    }
    if (b == 0) st8(v2u{(unsigned)tag, thi}, ar, lane == 0 ? phys(OFF_E) : OOB, 0);
    STAMP(2);
  }
  if (!A) { if (m < NA) { wait_rt(t_start + T_MLP); mlp_dma(p, lb, m, 4 + u, lane); } vmwait<0>(); }
  else if (u == 3) vmwait<18>();
  else if (u != 0) vmwait<8>();
  bar();
  STAMP(3);
  const int nc = __builtin_amdgcn_readfirstlane(*(LAS unsigned*)(L + L_M + 4)) * 8192;
  const v4u sel = selmask(lane);
  const v4u selone = sel & v4u{0x3f803f80u, 0x3f803f80u, 0x3f803f80u, 0x3f803f80u};
  const int rv = opq((2 * gq + (n >> 3)) * 1024 + u * 256 + (n & 7) * 16), rv3 = opq((2 * gq + (n >> 3)) < 5 ? rv : OOB);
  const int xw = opq(L_X + u * 256 + (n & 7) * 16 + (gq & 1) * 8);
  const int mlv = opq((n < 7) ? phys(OFF_ML + g * 2048) + (4 * n + gq) * 64 + hh * 8 : OOB);
  const int obv = opq(gq * 512 + (u - 1) * 256 + n * 16), ow = opq(L_O + (u - 1) * 256 + n * 16 + gq * 8), orr_ = opq(L_O + gq * 16);
  const int cv = opq(n == 0 ? phys(OFF_PC + (2 * g + hh) * 1024) + cb * 64 + gq * 16 : OOB);
  const int q2v = opq(n == 0 ? phys(OFF_Q2 + (g * 32 + r) * 64) + gq * 8 : OOB);
  const int qv = opq(phys(OFF_Q2 + g * 2048) + (lane >> 1) * 64 + (lane & 1) * 16), qw = opq(L_Q + hi * 576 + pos * 16);
  const bool xwr = n < 8 && gq < 2;

#pragma unroll 1
  for (int layer = 0; layer < 2; ++layer) {
    const int lo = layer * PLSTR;
    if (A) {
      if (layer == 0) { if (u == 3) vmwait<10>(); else vmwait<0>(); }
      bar();
      if (layer == 0) {
        if (u == 0) {
          const rsrc_t wr = mkrsrc(p.wo, 4096 * DM * 2);
          const int v0 = (lane >> 2) * (DM * 2) + (lane & 3) * 16;
#pragma unroll
          for (int k = 0; k < 16; ++k) dma16(lb + L_K + k * 1024, wr, v0, ((2 * g + hh) * 256 + 16 * k) * (DM * 2) + cb * 64);
        }
        if (u == 3) vmwait<0>();
        bar();
        if (u == 0) {
          vmwait<0>();
          const int a = L_K + (lane >> 2) * 64 + (lane & 3) * 8;
#pragma unroll
          for (int s = 0; s < 8; ++s) { wf[2 * s] = ltr2(L, a + s * 2048, 1024); wf[2 * s + 1] = ltr2(L, a + s * 2048 + 32, 1024); }
        }
      }
      if (u == 1 || u == 2) {
        const int obs = phys(OFF_OB + (g * 2 + hh) * 16384) + lo + nc;
        if (layer == 0) nap<DLY_B0>(); else nap<DLY_B1>();
        STAMP(4 + 6 * layer);
        v2u ml = ld8(ar, mlv, lo + nc);
        v4u rr[7];
#pragma unroll
        for (int s = 0; s < 7; ++s) rr[s] = ld16(ar, obv + (s & 1) * 2048, obs + (s >> 2) * 16384 + (s & 2) * 2048);
        f4 a; float inv;
        unsigned spin = 0;
        for (;; ++spin) {
          const float mp = n < 7 ? __uint_as_float(ml.x) : -__builtin_inff();
          const float mx = max_x32(max_x16(max16(mp)));
          const float sc = __builtin_amdgcn_exp2f(mp - mx);
          const unsigned scb = cvtpk(sc, sc);
          inv = __builtin_amdgcn_rcpf(sum_x32(sum_x16(sum16(sc * __uint_as_float(ml.y)))));
          a = f4{0.f, 0.f, 0.f, 0.f};
#define CSTEP(s) { const unsigned z = dppu<0x150 + s>(scb); a = mfma(sel & v4u{z, z, z, z}, rr[s], a); }
          CSTEP(0) CSTEP(1) CSTEP(2) CSTEP(3) CSTEP(4) CSTEP(5) CSTEP(6)
#undef CSTEP
          if (__builtin_expect(!__any(a.x != a.x || inv != inv), 1)) break;
          bool miss = max(ml.x, ml.y) == EMPTY;
          asm volatile("" ::: "memory");
          if (miss) ml = ld8(ar, mlv, lo + nc);
#pragma unroll
          for (int s = 0; s < 7; ++s) {
            const bool e = has_empty(rr[s]); miss |= e;
            if (e) rr[s] = ld16(ar, obv + (s & 1) * 2048, obs + (s >> 2) * 16384 + (s & 2) * 2048);
          }
          vmdrain();
          if (!__any(miss) || spin > SPIN_CAP) break;
        }
        if (gq < 2) *(LAS v2u*)(L + ow) = v2u{cvtpk(a.x * inv, a.y * inv), cvtpk(a.z * inv, a.w * inv)};
        STAMP(5 + 6 * layer);
      }
      bar();
      if (u == 0) {
        f4 c0 = {0.f, 0.f, 0.f, 0.f}, c1 = c0;
#pragma unroll
        for (int s = 0; s < 8; ++s) { const v4u of = lr16(L, orr_ + s * 64); c0 = mfma(wf[2 * s], of, c0); c1 = mfma(wf[2 * s + 1], of, c1); }
        pub16(v4u{cvtpk(c0.x, c0.y), cvtpk(c0.z, c0.w), cvtpk(c1.x, c1.y), cvtpk(c1.z, c1.w)}, ar, cv, lo);
        STAMP(6 + 6 * layer);
      }
    } else if (m < NA) {
      const v2u x0 = lr8(L, xw), x1 = lr8(L, u < 3 ? xw + 128 : xw);
      if (layer == 0) wait_rt(t_start + T_C0); else nap<DLY_C1>();
      STAMP(4 + 6 * layer);
      const int vo[2] = {rv, rv};
      if (u < 3) {
        f4 a[2]; sum_rows<2, 2>(ar, vo, phys(OFF_PC) + lo + nc, selone, a);
        if (xwr) {
          *(LAS v2u*)(L + xw) = v2u{cvtpk(a[0].x + bflo(x0.x), a[0].y + bfhi(x0.x)), cvtpk(a[0].z + bflo(x0.y), a[0].w + bfhi(x0.y))};
          *(LAS v2u*)(L + xw + 128) = v2u{cvtpk(a[1].x + bflo(x1.x), a[1].y + bfhi(x1.x)), cvtpk(a[1].z + bflo(x1.y), a[1].w + bfhi(x1.y))};
        }
      } else {
        f4 a[1]; sum_rows<2, 1>(ar, vo, phys(OFF_PC) + lo + nc, selone, a);
        if (xwr) *(LAS v2u*)(L + xw) = v2u{cvtpk(a[0].x + bflo(x0.x), a[0].y + bfhi(x0.x)), cvtpk(a[0].z + bflo(x0.y), a[0].w + bfhi(x0.y))};
      }
      STAMP(5 + 6 * layer);
      bar();
      if (u == 3 && m == 0) pub16(lr16(L, xl), ar, lane < 56 ? phys(OFF_PD + NA * 1024) + lane * 16 : OOB, lo);
      bar();
    }
    if (layer == 1) {
      if (A && b < 7 && u == 0) {
        nap<DLY_D>();
        STAMP(13);
        const int d = b * 128, vo[4] = {rv + d, rv + d, rv + d, rv3 + d};
        f4 a[1]; sum_rows<4, 1>(ar, vo, phys(OFF_PD) + lo + nc, selone, a);
        const int v = (b * 8 + (n & 7)) * 2 + (gq & 1);
        const rsrc_t orr = mkrsrc(p.out, DM * 2);
        __builtin_amdgcn_raw_buffer_store_b64(v2u{cvtpk(a[0].x, a[0].y), cvtpk(a[0].z, a[0].w)}, orr,
                                              xwr ? ((v >> 3) * 8 + (v & 1) * 4 + ((v >> 1) & 3)) * 8 : OOB, 0, 0);
        STAMP(15);
      }
      break;
    }
    if (A) nap<DLY_D>(); else if (m < NA) nap<DLY_DM>(); else wait_rt(t_start + T_C0 + 100);
    STAMP(7);
    {
      const int vo[4] = {rv, rv, rv, rv3};
      if (u < 3) {
        f4 a[2]; sum_rows<4, 2>(ar, vo, phys(OFF_PD) + nc, selone, a);
        if (xwr) {
          *(LAS v2u*)(L + xw) = v2u{cvtpk(a[0].x, a[0].y), cvtpk(a[0].z, a[0].w)};
          *(LAS v2u*)(L + xw + 128) = v2u{cvtpk(a[1].x, a[1].y), cvtpk(a[1].z, a[1].w)};
        }
      } else {
        f4 a[1]; sum_rows<4, 1>(ar, vo, phys(OFF_PD) + nc, selone, a);
        if (xwr) *(LAS v2u*)(L + xw) = v2u{cvtpk(a[0].x, a[0].y), cvtpk(a[0].z, a[0].w)};
      }
    }
    STAMP(8);
    bar();
    if (u == 3) {
      const v4u (&wq14)[14] = reinterpret_cast<const v4u (&)[14]>(wf);
      const f4 q = q_gemv(L, xa, xl, wq14, cs);
      pub8(v2u{cvtpk(q.x, q.y), cvtpk(q.z, q.w)}, ar, q2v, PLSTR);
      STAMP(9);
    }
    if (A) {
      if (u == 0) {
        nap<DLY_A>();
        v4u q; unsigned spin = 0;
        for (;; ++spin) {
          asm volatile("" ::: "memory");
          q = ld16(ar, qv, PLSTR + nc);
          if (!__any(has_empty(q)) || spin > SPIN_CAP) break;
        }
        *(LAS v4u*)(L + qw) = q;
        STAMP(9);
      }
      bar();
    } else if (m >= NA) break;
  }
}

__global__ void __launch_bounds__(512, 2) fused_decode(Params p) {
  __shared__ __attribute__((aligned(256))) unsigned char lds[163840];
  const unsigned long long t_start = rtc();
  p.arena = (unsigned char*)(((uintptr_t)p.arena + (ARENA - 1)) & ~(uintptr_t)(ARENA - 1));
  lptr L = (lptr)lds;
  const unsigned lb = (unsigned)(size_t)L;
  const int b = blockIdx.x, g = b & 7, r = b >> 3;
  const int lane = threadIdx.x & 63, w = __builtin_amdgcn_readfirstlane(threadIdx.x >> 6);
#define FIN() do { vmdrain(); __builtin_amdgcn_endpgm(); } while (0)
  if (r < NA) {
    if (w < 4) { attn_compute(p, L, lb, g, r, w, lane STAMP_PASS); FIN(); }
    else { service<true>(p, L, lb, b, g, r, w - 4, lane, t_start STAMP_PASS); FIN(); }
  } else {
    if (w < 4) { mlp_compute(p, L, lb, (r - NA) * 8 + g, w, lane, t_start STAMP_PASS); FIN(); }
    else { service<false>(p, L, lb, b, g, r, w - 4, lane, t_start STAMP_PASS); FIN(); }
  }
}

}

extern "C" void kernel_launch(void* const* d_in, const int* in_sizes, int n_in, void* d_out, int out_size, void* d_ws,
                              size_t ws_size, hipStream_t stream) {
  Params p;
  p.x = (const unsigned char*)d_in[0]; p.kc = (const unsigned char*)d_in[1]; p.vc = (const unsigned char*)d_in[2];
  p.wq = (const unsigned char*)d_in[3]; p.wo = (const unsigned char*)d_in[4];
  p.w1 = (const unsigned char*)d_in[6]; p.w2 = (const unsigned char*)d_in[7];
  p.out = (unsigned char*)d_out;
  p.arena = (unsigned char*)d_ws;
  fused_decode<<<256, 512, 0, stream>>>(p);
}
```

```cpp
#include <hip/hip_runtime.h>
#include <stdint.h>
#define ASM_STAMPS 0
#define WS_OFFSET(n) ((n) - ((size_t)8 << 20))

namespace {
struct Params {
  const void* x; const void* kc; const void* vc; const void* wq; const void* wo; const void* w1; const void* w2;
  void* out; void* ws;
};

__global__ void __launch_bounds__(512, 2) fused_decode(Params p) {
  __shared__ __attribute__((aligned(256))) unsigned char lds[163840];
  asm volatile(
    "; lds base %3\n"
    "\ts_load_dwordx16 s[8:23], %0, 0x0\n"
    "\ts_load_dwordx2 s[24:25], %0, 0x40\n"
    "\ts_memtime s[64:65]\n"
    "\ts_getpc_b64 s[6:7]\n"
    "L%=_pc:\n"
    "\tv_readfirstlane_b32 s4, %2\n"
    "\tv_and_b32 v1, 63, %2\n"
    "\ts_mov_b32 s53, %1\n"
    "\ts_and_b32 s26, %1, 7\n"
    "\ts_lshr_b32 s27, %1, 3\n"
    "\tv_and_b32 v6, 31, v1\n"
    "\ts_lshr_b32 s5, s4, 6\n"
    "\tv_lshlrev_b32 v3, 7, v6\n"
    "\ts_mul_i32 s4, s5, 2560\n"
    "\tv_add_u32 v3, s4, v3\n"
    "\tv_min_u32 v3, L%=_end-L%=_pc-4, v3\n"
    "\tglobal_load_dword v255, v3, s[6:7]\n"
    "\tv_and_b32 v2, 15, v1\n"
    "\tv_lshrrev_b32 v4, 4, v1\n"
    "\tv_lshrrev_b32 v5, 5, v1\n"
    "\ts_mul_i32 s49, s27, 149797\n"
    "\ts_lshr_b32 s49, s49, 10\n"
    "\ts_and_b32 s48, s5, 3\n"
    "\ts_lshl_b32 s49, s49, 9\n"
    "\ts_mov_b32 s50, 0\n"
    "\ts_mov_b32 s79, 8192\n"
    "\ts_mov_b32 s52, 0\n"
    "\ts_sub_u32 s54, s53, 224\n"
    "\ts_add_u32 s4, s27, 1\n"
    "\ts_mul_i32 s4, s4, 149797\n"
    "\ts_lshr_b32 s4, s4, 10\n"
    "\ts_lshl_b32 s4, s4, 9\n"
    "\ts_cmp_ge_u32 s5, 4\n"
    "\ts_cbranch_scc1 L%=_svc\n"
    "\ts_cmp_ge_u32 s27, 28\n"
    "\ts_cbranch_scc1 L%=_mlp\n"
    "\ts_lshl_b32 s56, s26, 25\n"
    "\ts_add_u32 s56, s56, 31457280\n"
    "\ts_mul_i32 s57, s26, 458752\n"
    "\ts_add_u32 s58, s57, 458748\n"
    "\ts_mov_b32 s59, 2097148\n"
    "\ts_mov_b32 s60, 1605628\n"
    "\ts_mov_b32 s61, 3670012\n"
    "\ts_waitcnt lgkmcnt(0)\n"
    "\ts_mov_b32 s74, s64\n"
    "\ts_cmp_lg_u32 s48, 0\n"
    "\ts_cbranch_scc1 L%=_tch1\n"
    "\ts_load_dword s6, s[10:11], s56\n"
    "L%=_tch1:\n"
    "\ts_cmp_lg_u32 s48, 1\n"
    "\ts_cbranch_scc1 L%=_tch2\n"
    "\ts_barrier\n"
    "\ts_sleep 10\n"
    "\ts_load_dword s6, s[12:13], s56\n"
    "\ts_load_dword s6, s[16:17], s57\n"
    "\ts_load_dword s6, s[16:17], s58\n"
    "\ts_load_dword s6, s[18:19], 0\n"
    "\ts_load_dword s6, s[18:19], s60\n"
    "\ts_load_dword s6, s[20:21], 0\n"
    "\ts_load_dword s6, s[20:21], s60\n"
    "\ts_load_dword s6, s[22:23], 0\n"
    "\ts_branch L%=_tch3\n"
    "L%=_tch2:\n"
    "\ts_barrier\n"
    "L%=_tch3:\n"
    "\ts_add_u32 s24, s24, 2097151\n"
    "\ts_addc_u32 s25, s25, 0\n"
    "\ts_and_b32 s24, s24, 4292870144\n"
    "\ts_mov_b32 s36, s24\n"
    "\ts_and_b32 s37, s25, 0xffff\n"
    "\ts_mov_b32 s38, 2097152\n"
    "\ts_mov_b32 s39, 0x20000\n"
    "\tv_and_b32 v3, 1, v1\n"
    "\tv_mul_u32_u24 v3, 0xaaaa, v3\n"
    "\tv_add_u32 v32, 0x4444, v3\n"
    "\ts_lshl_b32 s56, s48, 7\n"
    "\ts_add_u32 s56, s56, 160000\n"
    "\tv_bfe_u32 v3, v4, 0, 1\n"
    "\tv_lshlrev_b32 v22, 9, v3\n"
    "\tv_lshl_add_u32 v22, v5, 6, v22\n"
    "\tv_lshl_add_u32 v22, v2, 2, v22\n"
    "\tv_add_u32 v22, s56, v22\n"
    "\ts_and_b32 s57, s48, 1\n"
    "\ts_lshl_b32 s57, s57, 8\n"
    "\ts_lshr_b32 s56, s48, 1\n"
    "\ts_lshl_b32 s56, s56, 6\n"
    "\ts_add_u32 s56, s56, s57\n"
    "\ts_add_u32 s56, s56, 154112\n"
    "\tv_bfe_u32 v3, v4, 0, 1\n"
    "\tv_lshlrev_b32 v23, 7, v3\n"
    "\tv_lshl_add_u32 v23, v2, 2, v23\n"
    "\tv_add_u32 v23, s56, v23\n"
    "\tv_lshlrev_b32 v24, 2, v6\n"
    "\tv_add_u32 v24, 154624, v24\n"
    "\tv_cmp_eq_u32 vcc, 0, v5\n"
    "\ts_nop 1\n"
    "\tv_cndmask_b32 v23, v24, v23, vcc\n"
    "\tv_lshlrev_b32 v24, 9, v5\n"
    "\tv_lshl_add_u32 v24, v6, 2, v24\n"
    "\tv_add_u32 v24, 160000, v24\n"
    "\tv_lshlrev_b32 v25, 7, v5\n"
    "\tv_lshl_add_u32 v25, v6, 2, v25\n"
    "\tv_add_u32 v25, 154112, v25\n"
    "\tv_lshlrev_b32 v20, 5, v4\n"
    "\tv_bfe_u32 v3, v1, 0, 1\n"
    "\tv_lshl_add_u32 v20, v3, 4, v20\n"
    "\tv_bfe_u32 v3, v1, 2, 1\n"
    "\tv_lshl_add_u32 v20, v3, 9, v20\n"
    "\tv_bfe_u32 v3, v1, 2, 1\n"
    "\tv_lshl_add_u32 v20, v3, 7, v20\n"
    "\tv_add_u32 v20, 158720, v20\n"
    "\tv_add_u32 v21, s57, v20\n"
    "\ts_mul_i32 s58, s48, 640\n"
    "\ts_add_u32 s58, s58, 151552\n"
    "\ts_add_u32 s59, s58, 256\n"
    "\tv_lshlrev_b32 v28, 5, v4\n"
    "\tv_bfe_u32 v3, v1, 0, 1\n"
    "\tv_lshl_add_u32 v28, v3, 4, v28\n"
    "\tv_bfe_u32 v3, v1, 2, 1\n"
    "\tv_lshl_add_u32 v28, v3, 8, v28\n"
    "\tv_bfe_u32 v3, v1, 2, 1\n"
    "\tv_lshl_add_u32 v28, v3, 6, v28\n"
    "\tv_add_u32 v28, s58, v28\n"
    "\tv_lshlrev_b32 v29, 4, v4\n"
    "\tv_bfe_u32 v3, v1, 2, 1\n"
    "\tv_lshl_add_u32 v29, v3, 8, v29\n"
    "\tv_bfe_u32 v3, v1, 2, 1\n"
    "\tv_lshl_add_u32 v29, v3, 6, v29\n"
    "\tv_add_u32 v29, s59, v29\n"
    "\tv_and_b32 v36, 1, v1\n"
    "\tv_mov_b32 v7, 161088\n"
    "\tv_cmp_eq_u32 vcc, 0, v36\n"
    "\ts_nop 1\n"
    "\tv_cndmask_b32 v29, v7, v29, vcc\n"
    "\tv_lshlrev_b32 v26, 8, v5\n"
    "\tv_lshl_add_u32 v26, v5, 6, v26\n"
    "\tv_bfe_u32 v3, v6, 4, 1\n"
    "\tv_lshl_add_u32 v26, v3, 2, v26\n"
    "\tv_bfe_u32 v3, v6, 3, 1\n"
    "\tv_lshl_add_u32 v26, v3, 5, v26\n"
    "\tv_bfe_u32 v3, v6, 1, 1\n"
    "\tv_lshl_add_u32 v26, v3, 4, v26\n"
    "\tv_bfe_u32 v3, v6, 2, 1\n"
    "\tv_lshl_add_u32 v26, v3, 3, v26\n"
    "\tv_bfe_u32 v3, v6, 0, 1\n"
    "\tv_lshl_add_u32 v26, v3, 1, v26\n"
    "\tv_add_u32 v26, s58, v26\n"
    "\tv_lshlrev_b32 v27, 8, v5\n"
    "\tv_lshl_add_u32 v27, v5, 6, v27\n"
    "\tv_bfe_u32 v3, v6, 2, 2\n"
    "\tv_lshl_add_u32 v27, v3, 4, v27\n"
    "\tv_bfe_u32 v3, v6, 4, 1\n"
    "\tv_lshl_add_u32 v27, v3, 3, v27\n"
    "\tv_bfe_u32 v3, v6, 0, 2\n"
    "\tv_lshl_add_u32 v27, v3, 1, v27\n"
    "\tv_add_u32 v27, s59, v27\n"
    "\ts_lshl_b32 s56, s26, 6\n"
    "\ts_add_u32 s56, s56, s27\n"
    "\ts_lshl_b32 s56, s56, 9\n"
    "\ts_lshr_b32 s61, s56, 13\n"
    "\ts_lshl_b32 s61, s61, 13\n"
    "\ts_add_u32 s56, s56, s61\n"
    "\ts_lshl_b32 s57, s48, 7\n"
    "\ts_add_u32 s56, s56, s57\n"
    "\tv_lshlrev_b32 v30, 15, v4\n"
    "\tv_lshl_add_u32 v30, v2, 3, v30\n"
    "\tv_add_u32 v30, s56, v30\n"
    "\tv_mov_b32 v7, 1073741824\n"
    "\tv_cmp_gt_u32 vcc, 32, v1\n"
    "\ts_nop 1\n"
    "\tv_cndmask_b32 v30, v7, v30, vcc\n"
    "\ts_lshl_b32 s56, s26, 5\n"
    "\ts_add_u32 s56, s56, s27\n"
    "\ts_lshl_b32 s56, s56, 6\n"
    "\ts_lshr_b32 s61, s56, 13\n"
    "\ts_lshl_b32 s61, s61, 13\n"
    "\ts_add_u32 s56, s56, s61\n"
    "\ts_add_u32 s56, s56, 524288\n"
    "\ts_cmp_eq_u32 s48, 0\n"
    "\ts_cselect_b32 s56, s56, 1073741824\n"
    "\tv_lshlrev_b32 v31, 3, v5\n"
    "\tv_add_u32 v31, s56, v31\n"
    "\tv_mov_b32 v7, 1073741824\n"
    "\tv_cmp_eq_u32 vcc, 0, v6\n"
    "\ts_nop 1\n"
    "\tv_cndmask_b32 v31, v7, v31, vcc\n"
    "\ts_sub_u32 s56, s4, s49\n"
    "\ts_lshr_b32 s56, s56, 9\n"
    "\ts_sub_u32 s56, s56, 128\n"
    "\tv_cmp_gt_u32 s[66:67], s56, v6\n"
    "\ts_mov_b32 s68, 0\n"
    "\ts_mov_b32 s69, -1\n"
    "\tv_mov_b32 v33, 0xff800000\n"
    "\ts_mov_b32 s77, 0x3db8aa3b\n"
    "\ts_lshl_b32 s56, s48, 14\n"
    "\tv_xor_b32 v37, v4, v2\n"
    "\tv_lshlrev_b32 v36, 4, v37\n"
    "\tv_lshl_add_u32 v36, v2, 9, v36\n"
    "\tv_add_u32 v36, s56, v36\n"
    "\tv_xor_b32 v37, 64, v36\n"
    "\tv_xor_b32 v38, 128, v36\n"
    "\tv_xor_b32 v39, 192, v36\n"
    "\ts_lshr_b32 s56, s48, 1\n"
    "\ts_cmp_eq_u32 s56, 0\n"
    "\ts_cselect_b32 s57, 15, 3\n"
    "\tv_and_b32 v41, s57, v2\n"
    "\tv_xor_b32 v42, v4, v41\n"
    "\ts_lshl_b32 s56, s56, 13\n"
    "\ts_and_b32 s57, s48, 1\n"
    "\ts_lshl_b32 s57, s57, 8\n"
    "\ts_add_u32 s56, s56, s57\n"
    "\ts_add_u32 s56, s56, 65536\n"
    "\tv_lshlrev_b32 v40, 4, v42\n"
    "\tv_lshl_add_u32 v40, v41, 9, v40\n"
    "\tv_add_u32 v40, s56, v40\n"
    "\tv_xor_b32 v41, 64, v40\n"
    "\tv_xor_b32 v42, 128, v40\n"
    "\tv_xor_b32 v43, 192, v40\n"
    "\ts_lshl_b32 s56, s48, 7\n"
    "\tv_bfe_u32 v3, v1, 1, 1\n"
    "\tv_lshlrev_b32 v45, 4, v3\n"
    "\tv_add_u32 v45, s56, v45\n"
    "\tv_bfe_u32 v3, v1, 2, 3\n"
    "\tv_lshlrev_b32 v46, 5, v3\n"
    "\tv_xor_b32 v45, v45, v46\n"
    "\tv_bfe_u32 v3, v1, 0, 1\n"
    "\tv_lshlrev_b32 v44, 3, v3\n"
    "\tv_lshrrev_b32 v3, 2, v1\n"
    "\tv_lshl_add_u32 v44, v3, 9, v44\n"
    "\tv_lshl_add_u32 v44, v45, 0, v44\n"
    "\tv_add_u32 v44, 75776, v44\n"
    "\tv_bfe_u32 v3, v1, 0, 1\n"
    "\tv_lshlrev_b32 v48, 3, v3\n"
    "\tv_bfe_u32 v3, v1, 2, 2\n"
    "\tv_lshl_add_u32 v48, v3, 9, v48\n"
    "\tv_lshl_add_u32 v48, v45, 0, v48\n"
    "\tv_add_u32 v48, 149504, v48\n"
    "\tv_xor_b32 v45, 32, v44\n"
    "\tv_xor_b32 v49, 32, v48\n"
    "\tv_xor_b32 v46, 64, v44\n"
    "\tv_xor_b32 v50, 64, v48\n"
    "\tv_xor_b32 v47, 96, v44\n"
    "\tv_xor_b32 v51, 96, v48\n"
    "\tv_and_b32 v3, 1, v1\n"
    "\tv_lshlrev_b32 v3, 4, v3\n"
    "\tv_mov_b32 v7, 0xffff\n"
    "\tv_lshlrev_b32 v7, v3, v7\n"
    "\tv_lshrrev_b32 v3, 2, v2\n"
    "\tv_cmp_eq_u32 vcc, 0, v3\n"
    "\ts_nop 1\n"
    "\tv_cndmask_b32 v152, 0, v7, vcc\n"
    "\tv_and_b32 v156, 0x3f803f80, v152\n"
    "\tv_cmp_eq_u32 vcc, 1, v3\n"
    "\ts_nop 1\n"
    "\tv_cndmask_b32 v153, 0, v7, vcc\n"
    "\tv_and_b32 v157, 0x3f803f80, v153\n"
    "\tv_cmp_eq_u32 vcc, 2, v3\n"
    "\ts_nop 1\n"
    "\tv_cndmask_b32 v154, 0, v7, vcc\n"
    "\tv_and_b32 v158, 0x3f803f80, v154\n"
    "\tv_cmp_eq_u32 vcc, 3, v3\n"
    "\ts_nop 1\n"
    "\tv_cndmask_b32 v155, 0, v7, vcc\n"
    "\tv_and_b32 v159, 0x3f803f80, v155\n"
    "\ts_lshr_b32 s56, s48, 1\n"
    "\ts_mul_i32 s56, s56, 128\n"
    "\ts_lshl_b32 s57, s48, 8\n"
    "\ts_add_u32 s56, s56, s57\n"
    "\ts_add_u32 s56, s56, 158720\n"
    "\tv_lshrrev_b32 v3, 1, v2\n"
    "\tv_lshlrev_b32 v160, 5, v3\n"
    "\tv_bfe_u32 v3, v4, 0, 1\n"
    "\tv_lshl_add_u32 v160, v3, 4, v160\n"
    "\tv_bfe_u32 v3, v2, 0, 1\n"
    "\tv_lshl_add_u32 v160, v3, 3, v160\n"
    "\tv_lshl_add_u32 v160, v5, 2, v160\n"
    "\tv_add_u32 v160, s56, v160\n"
    "\ts_lshl_b32 s56, s26, 8\n"
    "\ts_lshl_b32 s57, s48, 15\n"
    "\ts_add_u32 s56, s56, s57\n"
    "\ts_add_u32 s56, s56, 1425408\n"
    "\tv_bfe_u32 v3, v2, 3, 1\n"
    "\tv_lshlrev_b32 v161, 14, v3\n"
    "\tv_lshl_add_u32 v161, v4, 11, v161\n"
    "\tv_bfe_u32 v3, v2, 0, 3\n"
    "\tv_lshl_add_u32 v161, v3, 5, v161\n"
    "\tv_add_u32 v161, s56, v161\n"
    "\tv_mov_b64 v[240:241], 0\n"
    "\tv_mov_b64 v[242:243], 0\n"
    "\tv_mov_b64 v[244:245], 0\n"
    "\tv_mov_b64 v[246:247], 0\n"
    "\tv_mov_b64 v[248:249], 0\n"
    "\tv_mov_b64 v[250:251], 0\n"
    "\tv_mov_b32 v3, 161052\n"
    "L%=_fw1:\n"
    "\ts_sleep 2\n"
    "\tds_read_b32 v7, v3\n"
    "\ts_waitcnt lgkmcnt(0)\n"
    "\tv_readfirstlane_b32 s61, v7\n"
    "\ts_nop 0\n"
    "\ts_cmp_lt_u32 s61, 1\n"
    "\ts_cbranch_scc1 L%=_fw1\n"
    "\ts_and_b32 s72, s61, -2\n"
    "\ts_mov_b32 s73, 0\n"
    "\ts_add_u32 s60, s74, 9000\n"
    "L%=_wc2:\n"
    "\ts_sleep 2\n"
    "\ts_memtime s[64:65]\n"
    "\ts_waitcnt lgkmcnt(0)\n"
    "\ts_sub_u32 s61, s64, s60\n"
    "\ts_cmp_lt_i32 s61, 0\n"
    "\ts_cbranch_scc1 L%=_wc2\n"
    "\tbuffer_load_dwordx4 v[136:139], v161, s[36:39], 0 offen offset:0 sc1\n"
    "\tbuffer_load_dwordx4 v[140:143], v161, s[36:39], 0 offen offset:16 sc1\n"
    "\tv_mov_b32 v3, 161040\n"
    "L%=_fw3:\n"
    "\ts_sleep 2\n"
    "\tds_read_b32 v7, v3\n"
    "\ts_waitcnt lgkmcnt(0)\n"
    "\tv_readfirstlane_b32 s61, v7\n"
    "\ts_nop 0\n"
    "\ts_cmp_lt_u32 s61, 1\n"
    "\ts_cbranch_scc1 L%=_fw3\n"
    "\tds_read_b128 v[56:59], v36 offset:0\n"
    "\tds_read_b128 v[60:63], v37 offset:0\n"
    "\tds_read_b128 v[64:67], v38 offset:0\n"
    "\tds_read_b128 v[68:71], v39 offset:0\n"
    "\tds_read_b128 v[72:75], v36 offset:256\n"
    "\tds_read_b128 v[76:79], v37 offset:256\n"
    "\tds_read_b128 v[80:83], v38 offset:256\n"
    "\tds_read_b128 v[84:87], v39 offset:256\n"
    "\ts_waitcnt lgkmcnt(4)\n"
    "\tds_read_b128 v[88:91], v36 offset:8192\n"
    "\tds_read_b128 v[92:95], v37 offset:8192\n"
    "\tds_read_b128 v[96:99], v38 offset:8192\n"
    "\tds_read_b128 v[100:103], v39 offset:8192\n"
    "\tds_read_b128 v[104:107], v36 offset:8448\n"
    "\tds_read_b128 v[108:111], v37 offset:8448\n"
    "\tds_read_b128 v[112:115], v38 offset:8448\n"
    "\tds_read_b128 v[116:119], v39 offset:8448\n"
    "\ts_waitcnt lgkmcnt(4)\n"
    "\tds_read_b128 v[120:123], v40\n"
    "\tds_read_b128 v[124:127], v41\n"
    "\tds_read_b128 v[128:131], v42\n"
    "\tds_read_b128 v[132:135], v43\n"
    "\ts_waitcnt lgkmcnt(0)\n"
    "\tv_mov_b32 v3, 161048\n"
    "\tv_mov_b32 v7, 1\n"
    "\ts_mov_b64 exec, 1\n"
    "\tds_add_u32 v3, v7\n"
    "\ts_mov_b64 exec, -1\n"
    "L%=_a_tag:\n"
    "\ts_waitcnt vmcnt(0)\n"
    "\ts_add_u32 s73, s73, 1\n"
    "\tv_xor_b32 v144, s72, v137\n"
    "\tv_xor_b32 v145, s72, v139\n"
    "\tv_xor_b32 v146, s72, v141\n"
    "\tv_or3_b32 v144, v144, v145, v146\n"
    "\tv_xor_b32 v145, s72, v143\n"
    "\tv_or_b32 v144, v144, v145\n"
    "\tv_cmp_ne_u32 vcc, 0, v144\n"
    "\ts_cbranch_vccz L%=_a_tagok\n"
    "\ts_cmp_ge_u32 s73, 40000\n"
    "\ts_cbranch_scc1 L%=_a_tagok\n"
    "\tbuffer_load_dwordx4 v[136:139], v161, s[36:39], 0 offen offset:0 sc1\n"
    "\tbuffer_load_dwordx4 v[140:143], v161, s[36:39], 0 offen offset:16 sc1\n"
    "\ts_branch L%=_a_tag\n"
    "L%=_a_tagok:\n"
    "\tv_mov_b32 v137, v138\n"
    "\tv_mov_b32 v138, v140\n"
    "\tv_mov_b32 v139, v142\n"
    "\ts_nop 1\n"
    "\tv_mfma_f32_16x16x32_bf16 v[144:147], v[156:159], v[136:139], 0\n"
    "\ts_nop 7\n"
    "\tv_cvt_pk_bf16_f32 v144, v144, v145\n"
    "\tds_write_b32 v160, v144\n"
    "\ts_waitcnt lgkmcnt(0)\n"
    "\tv_mov_b32 v3, 161056\n"
    "\tv_mov_b32 v7, 1\n"
    "\ts_mov_b64 exec, 1\n"
    "\tds_add_u32 v3, v7\n"
    "\ts_mov_b64 exec, -1\n"
    "\tv_mov_b32 v3, 161056\n"
    "L%=_fw4:\n"
    "\tds_read_b32 v7, v3\n"
    "\ts_waitcnt lgkmcnt(0)\n"
    "\tv_readfirstlane_b32 s61, v7\n"
    "\ts_nop 0\n"
    "\ts_cmp_lt_u32 s61, 4\n"
    "\ts_cbranch_scc1 L%=_fw4\n"
    "\ts_branch L%=_a_q\n"
    "L%=_aloop:\n"
    "\ts_barrier\n"
    "L%=_a_q:\n"
    "\tds_read_b128 v[232:235], v21\n"
    "\tds_read_b128 v[236:239], v21 offset:128\n"
    "\tds_read_b128 v[216:219], v20 offset:0\n"
    "\tds_read_b128 v[220:223], v20 offset:128\n"
    "\tds_read_b128 v[224:227], v20 offset:256\n"
    "\tds_read_b128 v[228:231], v20 offset:384\n"
    "\ts_waitcnt lgkmcnt(5)\n"
    "\tv_smfmac_f32_16x16x64_bf16 v[248:251], v[232:235], v[120:127], v32\n"
    "\ts_waitcnt lgkmcnt(4)\n"
    "\tv_smfmac_f32_16x16x64_bf16 v[248:251], v[236:239], v[128:135], v32\n"
    "\ts_waitcnt lgkmcnt(3)\n"
    "\tv_smfmac_f32_16x16x64_bf16 v[240:243], v[216:219], v[56:63], v32\n"
    "\tv_smfmac_f32_16x16x64_bf16 v[244:247], v[216:219], v[88:95], v32\n"
    "\ts_waitcnt lgkmcnt(2)\n"
    "\tv_smfmac_f32_16x16x64_bf16 v[240:243], v[220:223], v[64:71], v32\n"
    "\tv_smfmac_f32_16x16x64_bf16 v[244:247], v[220:223], v[96:103], v32\n"
    "\ts_waitcnt lgkmcnt(1)\n"
    "\tv_smfmac_f32_16x16x64_bf16 v[240:243], v[224:227], v[72:79], v32\n"
    "\tv_smfmac_f32_16x16x64_bf16 v[244:247], v[224:227], v[104:111], v32\n"
    "\tv_add_f32 v52, v248, v249\n"
    "\tds_write_b32 v23, v52\n"
    "\ts_waitcnt lgkmcnt(1)\n"
    "\tv_smfmac_f32_16x16x64_bf16 v[240:243], v[228:231], v[80:87], v32\n"
    "\tv_smfmac_f32_16x16x64_bf16 v[244:247], v[228:231], v[112:119], v32\n"
    "\ts_nop 6\n"
    "\tv_add_f32 v53, v240, v241\n"
    "\tv_add_f32 v54, v244, v245\n"
    "\tv_cndmask_b32 v53, v53, v54, s[68:69]\n"
    "\tds_write_b32 v22, v53\n"
    "\ts_waitcnt lgkmcnt(0)\n"
    "\ts_cmp_lg_u32 s52, 0\n"
    "\ts_cbranch_scc1 L%=_a_fs\n"
    "\tv_mov_b32 v3, 161060\n"
    "\tv_mov_b32 v7, 1\n"
    "\ts_mov_b64 exec, 1\n"
    "\tds_add_u32 v3, v7\n"
    "\ts_mov_b64 exec, -1\n"
    "\tv_mov_b32 v3, 161060\n"
    "L%=_fw5:\n"
    "\tds_read_b32 v7, v3\n"
    "\ts_waitcnt lgkmcnt(0)\n"
    "\tv_readfirstlane_b32 s61, v7\n"
    "\ts_nop 0\n"
    "\ts_cmp_lt_u32 s61, 4\n"
    "\ts_cbranch_scc1 L%=_fw5\n"
    "\ts_branch L%=_a_sm\n"
    "L%=_a_fs:\n"
    "\ts_barrier\n"
    "L%=_a_sm:\n"
    "\tds_read2_b32 v[216:217], v24 offset1:32\n"
    "\tds_read2_b32 v[218:219], v24 offset0:64 offset1:96\n"
    "\tds_read2st64_b32 v[220:221], v25 offset1:1\n"
    "\tv_mov_b64 v[240:241], 0\n"
    "\tv_mov_b64 v[242:243], 0\n"
    "\tv_mov_b64 v[244:245], 0\n"
    "\tv_mov_b64 v[246:247], 0\n"
    "\tv_mov_b64 v[248:249], 0\n"
    "\tv_mov_b64 v[250:251], 0\n"
    "\tv_mov_b64 v[252:253], 0\n"
    "\tv_mov_b64 v[254:255], 0\n"
    "\ts_waitcnt lgkmcnt(0)\n"
    "\tv_add_f32 v220, v220, v221\n"
    "\tv_max3_f32 v222, v216, v217, v218\n"
    "\tv_cndmask_b32 v220, v33, v220, s[66:67]\n"
    "\tv_max3_f32 v222, v222, v219, v220\n"
    "\ts_nop 1\n"
    "\tv_max_f32_dpp v222, v222, v222 quad_perm:[1,0,3,2] row_mask:0xf bank_mask:0xf\n"
    "\ts_nop 1\n"
    "\tv_max_f32_dpp v222, v222, v222 quad_perm:[2,3,0,1] row_mask:0xf bank_mask:0xf\n"
    "\ts_nop 1\n"
    "\tv_max_f32_dpp v222, v222, v222 row_half_mirror row_mask:0xf bank_mask:0xf\n"
    "\ts_nop 1\n"
    "\tv_max_f32_dpp v222, v222, v222 row_mirror row_mask:0xf bank_mask:0xf\n"
    "\tv_mov_b32 v221, v222\n"
    "\ts_nop 1\n"
    "\tv_permlane16_swap_b32 v222, v221\n"
    "\tv_max_f32 v222, v222, v221\n"
    "\tv_mul_f32 v222, s77, v222\n"
    "\tv_ceil_f32 v222, v222\n"
    "\tv_fma_f32 v216, v216, s77, -v222\n"
    "\tv_fma_f32 v217, v217, s77, -v222\n"
    "\tv_fma_f32 v218, v218, s77, -v222\n"
    "\tv_fma_f32 v219, v219, s77, -v222\n"
    "\tv_fma_f32 v220, v220, s77, -v222\n"
    "\tv_exp_f32 v216, v216\n"
    "\tv_exp_f32 v217, v217\n"
    "\tv_exp_f32 v218, v218\n"
    "\tv_exp_f32 v219, v219\n"
    "\tv_exp_f32 v220, v220\n"
    "\tv_cvt_pk_bf16_f32 v224, v216, v217\n"
    "\tv_cvt_pk_bf16_f32 v225, v218, v219\n"
    "\tv_cvt_pk_bf16_f32 v226, v220, v220\n"
    "\tds_write_b16 v26, v224\n"
    "\tds_write_b16_d16_hi v26, v224 offset:64\n"
    "\tds_write_b16 v26, v225 offset:128\n"
    "\tds_write_b16_d16_hi v26, v225 offset:192\n"
    "\tds_write_b16 v27, v226\n"
    "\tds_read_b128 v[228:231], v28\n"
    "\tds_read_b128 v[232:235], v28 offset:128\n"
    "\tds_read_b128 v[236:239], v29\n"
    "\ts_cmp_lg_u32 s52, 0\n"
    "\ts_cbranch_scc1 L%=_a_pv\n"
    "\ts_waitcnt lgkmcnt(0)\n"
    "\ts_barrier\n"
    "\tds_read_b64_tr_b16 v[136:137], v44 offset:0\n"
    "\tds_read_b64_tr_b16 v[138:139], v44 offset:8192\n"
    "\tds_read_b64_tr_b16 v[140:141], v44 offset:16384\n"
    "\tds_read_b64_tr_b16 v[142:143], v44 offset:24576\n"
    "\tds_read_b64_tr_b16 v[144:145], v44 offset:32768\n"
    "\tds_read_b64_tr_b16 v[146:147], v44 offset:40960\n"
    "\tds_read_b64_tr_b16 v[148:149], v44 offset:49152\n"
    "\tds_read_b64_tr_b16 v[150:151], v44 offset:57344\n"
    "\ts_waitcnt lgkmcnt(4)\n"
    "\tds_read_b64_tr_b16 v[152:153], v45 offset:0\n"
    "\tds_read_b64_tr_b16 v[154:155], v45 offset:8192\n"
    "\tds_read_b64_tr_b16 v[156:157], v45 offset:16384\n"
    "\tds_read_b64_tr_b16 v[158:159], v45 offset:24576\n"
    "\tds_read_b64_tr_b16 v[160:161], v45 offset:32768\n"
    "\tds_read_b64_tr_b16 v[162:163], v45 offset:40960\n"
    "\tds_read_b64_tr_b16 v[164:165], v45 offset:49152\n"
    "\tds_read_b64_tr_b16 v[166:167], v45 offset:57344\n"
    "\ts_waitcnt lgkmcnt(4)\n"
    "\tds_read_b64_tr_b16 v[168:169], v46 offset:0\n"
    "\tds_read_b64_tr_b16 v[170:171], v46 offset:8192\n"
    "\tds_read_b64_tr_b16 v[172:173], v46 offset:16384\n"
    "\tds_read_b64_tr_b16 v[174:175], v46 offset:24576\n"
    "\tds_read_b64_tr_b16 v[176:177], v46 offset:32768\n"
    "\tds_read_b64_tr_b16 v[178:179], v46 offset:40960\n"
    "\tds_read_b64_tr_b16 v[180:181], v46 offset:49152\n"
    "\tds_read_b64_tr_b16 v[182:183], v46 offset:57344\n"
    "\ts_waitcnt lgkmcnt(4)\n"
    "\tds_read_b64_tr_b16 v[184:185], v47 offset:0\n"
    "\tds_read_b64_tr_b16 v[186:187], v47 offset:8192\n"
    "\tds_read_b64_tr_b16 v[188:189], v47 offset:16384\n"
    "\tds_read_b64_tr_b16 v[190:191], v47 offset:24576\n"
    "\tds_read_b64_tr_b16 v[192:193], v47 offset:32768\n"
    "\tds_read_b64_tr_b16 v[194:195], v47 offset:40960\n"
    "\tds_read_b64_tr_b16 v[196:197], v47 offset:49152\n"
    "\tds_read_b64_tr_b16 v[198:199], v47 offset:57344\n"
    "\ts_waitcnt lgkmcnt(4)\n"
    "\ts_waitcnt lgkmcnt(0)\n"
    "L%=_a_pv:\n"
    "\tv_add_f32 v223, v216, v217\n"
    "\tv_add_f32 v218, v218, v219\n"
    "\tv_add_f32 v223, v223, v220\n"
    "\tv_add_f32 v223, v223, v218\n"
    "\ts_waitcnt lgkmcnt(2)\n"
    "\tv_smfmac_f32_16x16x64_bf16 v[240:243], v[228:231], v[136:143], v32\n"
    "\tv_add_f32_dpp v223, v223, v223 quad_perm:[1,0,3,2] row_mask:0xf bank_mask:0xf\n"
    "\tv_smfmac_f32_16x16x64_bf16 v[244:247], v[228:231], v[152:159], v32\n"
    "\ts_nop 0\n"
    "\tv_add_f32_dpp v223, v223, v223 quad_perm:[2,3,0,1] row_mask:0xf bank_mask:0xf\n"
    "\tv_smfmac_f32_16x16x64_bf16 v[248:251], v[228:231], v[168:175], v32\n"
    "\ts_nop 0\n"
    "\tv_add_f32_dpp v223, v223, v223 row_half_mirror row_mask:0xf bank_mask:0xf\n"
    "\tv_smfmac_f32_16x16x64_bf16 v[252:255], v[228:231], v[184:191], v32\n"
    "\ts_nop 0\n"
    "\tv_add_f32_dpp v223, v223, v223 row_mirror row_mask:0xf bank_mask:0xf\n"
    "\ts_waitcnt lgkmcnt(1)\n"
    "\tv_smfmac_f32_16x16x64_bf16 v[240:243], v[232:235], v[144:151], v32\n"
    "\tv_mov_b32 v221, v223\n"
    "\tv_smfmac_f32_16x16x64_bf16 v[244:247], v[232:235], v[160:167], v32\n"
    "\ts_nop 0\n"
    "\tv_permlane16_swap_b32 v223, v221\n"
    "\tv_smfmac_f32_16x16x64_bf16 v[248:251], v[232:235], v[176:183], v32\n"
    "\tv_add_f32 v223, v223, v221\n"
    "\tv_smfmac_f32_16x16x64_bf16 v[252:255], v[232:235], v[192:199], v32\n"
    "\tbuffer_store_dwordx2 v[222:223], v31, s[36:39], s50 offen sc1\n"
    "\tbuffer_store_dwordx2 v[222:223], v31, s[36:39], s79 offen sc1\n"
    "\ts_waitcnt lgkmcnt(0)\n"
    "\ts_cmp_lg_u32 s52, 0\n"
    "\ts_cbranch_scc1 L%=_a_p4\n"
    "\tv_add_u32 v44, 0x10000, v44\n"
    "\tv_add_u32 v45, 0x10000, v45\n"
    "\tv_add_u32 v46, 0x10000, v46\n"
    "\tv_add_u32 v47, 0x10000, v47\n"
    "\ts_barrier\n"
    "\tds_read_b64_tr_b16 v[200:201], v44\n"
    "\tds_read_b64_tr_b16 v[202:203], v48\n"
    "\tds_read_b64_tr_b16 v[204:205], v45\n"
    "\tds_read_b64_tr_b16 v[206:207], v49\n"
    "\tds_read_b64_tr_b16 v[208:209], v46\n"
    "\tds_read_b64_tr_b16 v[210:211], v50\n"
    "\tds_read_b64_tr_b16 v[212:213], v47\n"
    "\tds_read_b64_tr_b16 v[214:215], v51\n"
    "L%=_a_p4:\n"
    "\ts_waitcnt lgkmcnt(6)\n"
    "\tv_mfma_f32_16x16x32_bf16 v[240:243], v[236:239], v[200:203], v[240:243]\n"
    "\ts_waitcnt lgkmcnt(4)\n"
    "\tv_mfma_f32_16x16x32_bf16 v[244:247], v[236:239], v[204:207], v[244:247]\n"
    "\ts_waitcnt lgkmcnt(2)\n"
    "\tv_mfma_f32_16x16x32_bf16 v[248:251], v[236:239], v[208:211], v[248:251]\n"
    "\ts_waitcnt lgkmcnt(0)\n"
    "\tv_mfma_f32_16x16x32_bf16 v[252:255], v[236:239], v[212:215], v[252:255]\n"
    "\ts_nop 1\n"
    "\tv_add_f32 v216, v240, v241\n"
    "\ts_nop 0\n"
    "\tv_add_f32 v217, v244, v245\n"
    "\ts_nop 0\n"
    "\tv_add_f32 v218, v248, v249\n"
    "\ts_nop 0\n"
    "\tv_add_f32 v219, v252, v253\n"
    "\tv_cvt_pk_bf16_f32 v216, v216, v217\n"
    "\tv_cvt_pk_bf16_f32 v217, v218, v219\n"
    "\tbuffer_store_dwordx2 v[216:217], v30, s[36:39], s50 offen sc1\n"
    "\tbuffer_store_dwordx2 v[216:217], v30, s[36:39], s79 offen sc1\n"
    "\ts_barrier\n"
    "\ts_cmp_lg_u32 s52, 0\n"
    "\ts_cbranch_scc1 L%=_exit\n"
    "\tv_mov_b64 v[240:241], 0\n"
    "\tv_mov_b64 v[242:243], 0\n"
    "\tv_mov_b64 v[244:245], 0\n"
    "\tv_mov_b64 v[246:247], 0\n"
    "\tv_mov_b64 v[248:249], 0\n"
    "\tv_mov_b64 v[250:251], 0\n"
    "\ts_mov_b32 s52, 1\n"
    "\ts_mov_b32 s50, 655360\n"
    "\ts_mov_b32 s79, 663552\n"
    "\ts_barrier\n"
    "\ts_barrier\n"
    "\ts_branch L%=_aloop\n"
    "L%=_mlp:\n"
    "\ts_waitcnt lgkmcnt(0)\n"
    "\ts_mov_b32 s74, s64\n"
    "\ts_add_u32 s24, s24, 2097151\n"
    "\ts_addc_u32 s25, s25, 0\n"
    "\ts_and_b32 s24, s24, 4292870144\n"
    "\ts_mov_b32 s36, s24\n"
    "\ts_and_b32 s37, s25, 0xffff\n"
    "\ts_mov_b32 s38, 2097152\n"
    "\ts_mov_b32 s39, 0x20000\n"
    "\ts_barrier\n"
    "\ts_cmp_ge_u32 s54, 28\n"
    "\ts_cbranch_scc1 L%=_exit\n"
    "\ts_mov_b32 s40, s18\n"
    "\ts_and_b32 s41, s19, 0xffff\n"
    "\ts_mov_b32 s42, 1605632\n"
    "\ts_mov_b32 s43, 0x20000\n"
    "\ts_mov_b32 s44, s20\n"
    "\ts_and_b32 s45, s21, 0xffff\n"
    "\ts_mov_b32 s46, 1605632\n"
    "\ts_mov_b32 s47, 0x20000\n"
    "\tv_lshrrev_b32 v3, 3, v1\n"
    "\tv_mul_u32_u24 v8, 3584, v3\n"
    "\tv_lshlrev_b32 v7, 1, v5\n"
    "\tv_and_b32 v3, 7, v1\n"
    "\tv_xor_b32 v3, v3, v7\n"
    "\tv_lshlrev_b32 v10, 4, v3\n"
    "\tv_lshlrev_b32 v9, 4, v1\n"
    "\ts_mul_i32 s56, s5, 7\n"
    "\ts_lshl_b32 m0, s56, 10\n"
    "\ts_mul_i32 s57, s5, 200704\n"
    "\ts_lshl_b32 s58, s54, 7\n"
    "\ts_add_u32 s57, s57, s58\n"
    "\ts_add_u32 s60, s74, 8000\n"
    "L%=_wc6:\n"
    "\ts_sleep 2\n"
    "\ts_memtime s[64:65]\n"
    "\ts_waitcnt lgkmcnt(0)\n"
    "\ts_sub_u32 s61, s64, s60\n"
    "\ts_cmp_lt_i32 s61, 0\n"
    "\ts_cbranch_scc1 L%=_wc6\n"
    "\ts_and_b32 s58, s56, 2\n"
    "\ts_lshl_b32 s58, s58, 5\n"
    "\tv_xor_b32 v3, s58, v10\n"
    "\tv_add_u32 v3, v3, v8\n"
    "\tbuffer_load_dwordx4 v3, s[40:43], s57 offen nt lds\n"
    "\ts_waitcnt vmcnt(3)\n"
    "\ts_add_u32 m0, m0, 1024\n"
    "\ts_add_u32 s57, s57, 28672\n"
    "\ts_add_u32 s56, s56, 1\n"
    "\ts_and_b32 s58, s56, 2\n"
    "\ts_lshl_b32 s58, s58, 5\n"
    "\tv_xor_b32 v3, s58, v10\n"
    "\tv_add_u32 v3, v3, v8\n"
    "\tbuffer_load_dwordx4 v3, s[40:43], s57 offen nt lds\n"
    "\ts_waitcnt vmcnt(3)\n"
    "\ts_add_u32 m0, m0, 1024\n"
    "\ts_add_u32 s57, s57, 28672\n"
    "\ts_add_u32 s56, s56, 1\n"
    "\ts_and_b32 s58, s56, 2\n"
    "\ts_lshl_b32 s58, s58, 5\n"
    "\tv_xor_b32 v3, s58, v10\n"
    "\tv_add_u32 v3, v3, v8\n"
    "\tbuffer_load_dwordx4 v3, s[40:43], s57 offen nt lds\n"
    "\ts_waitcnt vmcnt(3)\n"
    "\ts_add_u32 m0, m0, 1024\n"
    "\ts_add_u32 s57, s57, 28672\n"
    "\ts_add_u32 s56, s56, 1\n"
    "\ts_and_b32 s58, s56, 2\n"
    "\ts_lshl_b32 s58, s58, 5\n"
    "\tv_xor_b32 v3, s58, v10\n"
    "\tv_add_u32 v3, v3, v8\n"
    "\tbuffer_load_dwordx4 v3, s[40:43], s57 offen nt lds\n"
    "\ts_waitcnt vmcnt(3)\n"
    "\ts_add_u32 m0, m0, 1024\n"
    "\ts_add_u32 s57, s57, 28672\n"
    "\ts_add_u32 s56, s56, 1\n"
    "\ts_and_b32 s58, s56, 2\n"
    "\ts_lshl_b32 s58, s58, 5\n"
    "\tv_xor_b32 v3, s58, v10\n"
    "\tv_add_u32 v3, v3, v8\n"
    "\tbuffer_load_dwordx4 v3, s[40:43], s57 offen nt lds\n"
    "\ts_waitcnt vmcnt(3)\n"
    "\ts_add_u32 m0, m0, 1024\n"
    "\ts_add_u32 s57, s57, 28672\n"
    "\ts_add_u32 s56, s56, 1\n"
    "\ts_and_b32 s58, s56, 2\n"
    "\ts_lshl_b32 s58, s58, 5\n"
    "\tv_xor_b32 v3, s58, v10\n"
    "\tv_add_u32 v3, v3, v8\n"
    "\tbuffer_load_dwordx4 v3, s[40:43], s57 offen nt lds\n"
    "\ts_waitcnt vmcnt(3)\n"
    "\ts_add_u32 m0, m0, 1024\n"
    "\ts_add_u32 s57, s57, 28672\n"
    "\ts_add_u32 s56, s56, 1\n"
    "\ts_and_b32 s58, s56, 2\n"
    "\ts_lshl_b32 s58, s58, 5\n"
    "\tv_xor_b32 v3, s58, v10\n"
    "\tv_add_u32 v3, v3, v8\n"
    "\tbuffer_load_dwordx4 v3, s[40:43], s57 offen nt lds\n"
    "\ts_waitcnt vmcnt(3)\n"
    "\ts_mul_i32 s56, s5, 7424\n"
    "\ts_add_u32 m0, s56, 57344\n"
    "\ts_mul_i32 s58, s54, 57344\n"
    "\ts_mul_i32 s56, s5, 7168\n"
    "\ts_add_u32 s58, s58, s56\n"
    "\ts_mov_b32 exec_hi, 0xffffff\n"
    "\tbuffer_load_dwordx4 v9, s[44:47], s58 offen nt lds\n"
    "\ts_waitcnt vmcnt(3)\n"
    "\ts_add_u32 m0, m0, 928\n"
    "\ts_add_u32 s58, s58, 896\n"
    "\tbuffer_load_dwordx4 v9, s[44:47], s58 offen nt lds\n"
    "\ts_waitcnt vmcnt(3)\n"
    "\ts_add_u32 m0, m0, 928\n"
    "\ts_add_u32 s58, s58, 896\n"
    "\tbuffer_load_dwordx4 v9, s[44:47], s58 offen nt lds\n"
    "\ts_waitcnt vmcnt(3)\n"
    "\ts_add_u32 m0, m0, 928\n"
    "\ts_add_u32 s58, s58, 896\n"
    "\tbuffer_load_dwordx4 v9, s[44:47], s58 offen nt lds\n"
    "\ts_waitcnt vmcnt(3)\n"
    "\ts_add_u32 m0, m0, 928\n"
    "\ts_add_u32 s58, s58, 896\n"
    "\tbuffer_load_dwordx4 v9, s[44:47], s58 offen nt lds\n"
    "\ts_waitcnt vmcnt(3)\n"
    "\ts_add_u32 m0, m0, 928\n"
    "\ts_add_u32 s58, s58, 896\n"
    "\tbuffer_load_dwordx4 v9, s[44:47], s58 offen nt lds\n"
    "\ts_waitcnt vmcnt(3)\n"
    "\ts_add_u32 m0, m0, 928\n"
    "\ts_add_u32 s58, s58, 896\n"
    "\tbuffer_load_dwordx4 v9, s[44:47], s58 offen nt lds\n"
    "\ts_waitcnt vmcnt(3)\n"
    "\ts_add_u32 m0, m0, 928\n"
    "\ts_add_u32 s58, s58, 896\n"
    "\tbuffer_load_dwordx4 v9, s[44:47], s58 offen nt lds\n"
    "\ts_waitcnt vmcnt(3)\n"
    "\ts_mov_b32 exec_hi, -1\n"
    "\tv_and_b32 v3, 1, v1\n"
    "\tv_mul_u32_u24 v3, 0xaaaa, v3\n"
    "\tv_add_u32 v25, 0x4444, v3\n"
    "\tv_lshlrev_b32 v20, 5, v4\n"
    "\tv_bfe_u32 v3, v1, 0, 1\n"
    "\tv_lshl_add_u32 v20, v3, 4, v20\n"
    "\tv_add_u32 v20, 157696, v20\n"
    "\tv_lshlrev_b32 v21, 4, v1\n"
    "\tv_add_u32 v21, 157696, v21\n"
    "\ts_lshr_b32 s57, s48, 1\n"
    "\ts_lshl_b32 s57, s57, 6\n"
    "\ts_and_b32 s56, s48, 1\n"
    "\ts_lshl_b32 s56, s56, 2\n"
    "\ts_add_u32 s57, s57, s56\n"
    "\ts_add_u32 s57, s57, 155392\n"
    "\tv_bfe_u32 v3, v2, 3, 1\n"
    "\tv_lshlrev_b32 v22, 5, v3\n"
    "\tv_bfe_u32 v3, v2, 1, 1\n"
    "\tv_lshl_add_u32 v22, v3, 4, v22\n"
    "\tv_bfe_u32 v3, v2, 2, 1\n"
    "\tv_lshl_add_u32 v22, v3, 3, v22\n"
    "\tv_bfe_u32 v3, v2, 0, 1\n"
    "\tv_lshl_add_u32 v22, v3, 1, v22\n"
    "\tv_add_u32 v22, s57, v22\n"
    "\tv_lshlrev_b32 v30, 1, v1\n"
    "\tv_add_u32 v30, 154624, v30\n"
    "\tv_cmp_gt_u32 vcc, 16, v1\n"
    "\ts_nop 1\n"
    "\tv_cndmask_b32 v22, v30, v22, vcc\n"
    "\tv_lshlrev_b32 v30, 5, v4\n"
    "\tv_bfe_u32 v3, v1, 0, 1\n"
    "\tv_lshl_add_u32 v30, v3, 4, v30\n"
    "\tv_add_u32 v30, 155392, v30\n"
    "\tv_lshrrev_b32 v3, 2, v2\n"
    "\tv_mov_b32 v7, 161088\n"
    "\tv_cmp_eq_u32 vcc, 0, v3\n"
    "\ts_nop 1\n"
    "\tv_cndmask_b32 v26, v7, v30, vcc\n"
    "\tv_cmp_eq_u32 vcc, 1, v3\n"
    "\ts_nop 1\n"
    "\tv_cndmask_b32 v27, v7, v30, vcc\n"
    "\tv_cmp_eq_u32 vcc, 2, v3\n"
    "\ts_nop 1\n"
    "\tv_cndmask_b32 v28, v7, v30, vcc\n"
    "\tv_cmp_eq_u32 vcc, 3, v3\n"
    "\ts_nop 1\n"
    "\tv_cndmask_b32 v29, v7, v30, vcc\n"
    "\tv_lshlrev_b32 v30, 9, v4\n"
    "\tv_bfe_u32 v3, v1, 3, 1\n"
    "\tv_lshl_add_u32 v30, v3, 7, v30\n"
    "\tv_bfe_u32 v3, v1, 2, 1\n"
    "\tv_lshl_add_u32 v30, v3, 11, v30\n"
    "\tv_bfe_u32 v3, v1, 0, 2\n"
    "\tv_lshl_add_u32 v30, v3, 3, v30\n"
    "\tv_bfe_u32 v3, v4, 0, 1\n"
    "\tv_lshl_add_u32 v30, v3, 5, v30\n"
    "\tv_bfe_u32 v3, v1, 2, 1\n"
    "\tv_lshl_add_u32 v30, v3, 6, v30\n"
    "\ts_lshl_b32 s56, s48, 5\n"
    "\tv_xor_b32 v30, s56, v30\n"
    "\ts_lshl_b32 s56, s48, 2\n"
    "\ts_mul_i32 s57, s48, 3\n"
    "\ts_add_u32 s57, s57, 2\n"
    "\ts_cmp_lt_u32 s48, 2\n"
    "\ts_cselect_b32 s57, s56, s57\n"
    "\ts_cselect_b32 s59, 64, 48\n"
    "\ts_lshl_b32 s57, s57, 6\n"
    "\ts_lshl_b32 s56, s54, 10\n"
    "\ts_lshr_b32 s61, s56, 13\n"
    "\ts_lshl_b32 s61, s61, 13\n"
    "\ts_add_u32 s56, s56, s61\n"
    "\ts_add_u32 s56, s56, 589824\n"
    "\ts_add_u32 s56, s56, s57\n"
    "\ts_add_u32 s57, s57, 57344\n"
    "\tv_lshlrev_b32 v23, 2, v1\n"
    "\tv_add_u32 v23, s56, v23\n"
    "\tv_mov_b32 v7, 1073741824\n"
    "\tv_cmp_gt_u32 vcc, s59, v1\n"
    "\ts_nop 1\n"
    "\tv_cndmask_b32 v23, v7, v23, vcc\n"
    "\tv_lshrrev_b32 v3, 2, v1\n"
    "\tv_lshlrev_b32 v32, 0, v3\n"
    "\tv_mul_u32_u24 v32, 928, v32\n"
    "\tv_bfe_u32 v3, v1, 0, 2\n"
    "\tv_lshlrev_b32 v31, 3, v3\n"
    "\tv_lshl_add_u32 v31, v32, 0, v31\n"
    "\tv_add_u32 v31, s57, v31\n"
    "\ts_waitcnt vmcnt(0)\n"
    "\ts_barrier\n"
    "\tds_read_b64_tr_b16 v[56:57], v30 offset:0\n"
    "\tds_read_b64_tr_b16 v[58:59], v30 offset:256\n"
    "\tds_read_b64_tr_b16 v[60:61], v30 offset:4096\n"
    "\tds_read_b64_tr_b16 v[62:63], v30 offset:4352\n"
    "\tds_read_b64_tr_b16 v[64:65], v30 offset:8192\n"
    "\tds_read_b64_tr_b16 v[66:67], v30 offset:8448\n"
    "\tds_read_b64_tr_b16 v[68:69], v30 offset:12288\n"
    "\tds_read_b64_tr_b16 v[70:71], v30 offset:12544\n"
    "\ts_waitcnt lgkmcnt(4)\n"
    "\tds_read_b64_tr_b16 v[72:73], v30 offset:16384\n"
    "\tds_read_b64_tr_b16 v[74:75], v30 offset:16640\n"
    "\tds_read_b64_tr_b16 v[76:77], v30 offset:20480\n"
    "\tds_read_b64_tr_b16 v[78:79], v30 offset:20736\n"
    "\tds_read_b64_tr_b16 v[80:81], v30 offset:24576\n"
    "\tds_read_b64_tr_b16 v[82:83], v30 offset:24832\n"
    "\tds_read_b64_tr_b16 v[84:85], v30 offset:28672\n"
    "\tds_read_b64_tr_b16 v[86:87], v30 offset:28928\n"
    "\ts_waitcnt lgkmcnt(4)\n"
    "\tds_read_b64_tr_b16 v[88:89], v30 offset:32768\n"
    "\tds_read_b64_tr_b16 v[90:91], v30 offset:33024\n"
    "\tds_read_b64_tr_b16 v[92:93], v30 offset:36864\n"
    "\tds_read_b64_tr_b16 v[94:95], v30 offset:37120\n"
    "\tds_read_b64_tr_b16 v[96:97], v30 offset:40960\n"
    "\tds_read_b64_tr_b16 v[98:99], v30 offset:41216\n"
    "\tds_read_b64_tr_b16 v[100:101], v30 offset:45056\n"
    "\tds_read_b64_tr_b16 v[102:103], v30 offset:45312\n"
    "\ts_waitcnt lgkmcnt(4)\n"
    "\tds_read_b64_tr_b16 v[104:105], v30 offset:49152\n"
    "\tds_read_b64_tr_b16 v[106:107], v30 offset:49408\n"
    "\tds_read_b64_tr_b16 v[108:109], v30 offset:53248\n"
    "\tds_read_b64_tr_b16 v[110:111], v30 offset:53504\n"
    "\ts_cmp_eq_u32 s48, 3\n"
    "\ts_cselect_b32 s56, 64, 0\n"
    "\tv_subrev_u32 v32, s56, v31\n"
    "\tds_read_b64_tr_b16 v[112:113], v31 offset:0\n"
    "\tds_read_b64_tr_b16 v[114:115], v31 offset:14848\n"
    "\tds_read_b64_tr_b16 v[116:117], v31 offset:29696\n"
    "\tds_read_b64_tr_b16 v[118:119], v31 offset:44544\n"
    "\tds_read_b64_tr_b16 v[120:121], v31 offset:32\n"
    "\tds_read_b64_tr_b16 v[122:123], v31 offset:14880\n"
    "\tds_read_b64_tr_b16 v[124:125], v31 offset:29728\n"
    "\tds_read_b64_tr_b16 v[126:127], v31 offset:44576\n"
    "\ts_waitcnt lgkmcnt(4)\n"
    "\tds_read_b64_tr_b16 v[128:129], v31 offset:64\n"
    "\tds_read_b64_tr_b16 v[130:131], v31 offset:14912\n"
    "\tds_read_b64_tr_b16 v[132:133], v31 offset:29760\n"
    "\tds_read_b64_tr_b16 v[134:135], v31 offset:44608\n"
    "\tds_read_b64_tr_b16 v[136:137], v31 offset:96\n"
    "\tds_read_b64_tr_b16 v[138:139], v31 offset:14944\n"
    "\tds_read_b64_tr_b16 v[140:141], v31 offset:29792\n"
    "\tds_read_b64_tr_b16 v[142:143], v31 offset:44640\n"
    "\ts_waitcnt lgkmcnt(4)\n"
    "\tds_read_b64_tr_b16 v[144:145], v31 offset:128\n"
    "\tds_read_b64_tr_b16 v[146:147], v31 offset:14976\n"
    "\tds_read_b64_tr_b16 v[148:149], v31 offset:29824\n"
    "\tds_read_b64_tr_b16 v[150:151], v31 offset:44672\n"
    "\tds_read_b64_tr_b16 v[152:153], v31 offset:160\n"
    "\tds_read_b64_tr_b16 v[154:155], v31 offset:15008\n"
    "\tds_read_b64_tr_b16 v[156:157], v31 offset:29856\n"
    "\tds_read_b64_tr_b16 v[158:159], v31 offset:44704\n"
    "\ts_waitcnt lgkmcnt(4)\n"
    "\tds_read_b64_tr_b16 v[160:161], v32 offset:192\n"
    "\tds_read_b64_tr_b16 v[162:163], v32 offset:15040\n"
    "\tds_read_b64_tr_b16 v[164:165], v32 offset:29888\n"
    "\tds_read_b64_tr_b16 v[166:167], v32 offset:44736\n"
    "\tds_read_b64_tr_b16 v[168:169], v32 offset:224\n"
    "\tds_read_b64_tr_b16 v[170:171], v32 offset:15072\n"
    "\tds_read_b64_tr_b16 v[172:173], v32 offset:29920\n"
    "\tds_read_b64_tr_b16 v[174:175], v32 offset:44768\n"
    "\ts_waitcnt lgkmcnt(4)\n"
    "\tv_mov_b32 v204, 0x3f803f80\n"
    "\tv_mov_b32 v205, 0x3f803f80\n"
    "\tv_mov_b32 v206, 0x3f803f80\n"
    "\tv_mov_b32 v207, 0x3f803f80\n"
    "\tv_mov_b64 v[240:241], 0\n"
    "\tv_mov_b64 v[242:243], 0\n"
    "\ts_waitcnt lgkmcnt(0)\n"
    "\ts_nop 0\n"
    "\tv_smfmac_f32_16x16x64_bf16 v[240:243], v[204:207], v[56:63], v25\n"
    "\tv_smfmac_f32_16x16x64_bf16 v[240:243], v[204:207], v[64:71], v25\n"
    "\tv_smfmac_f32_16x16x64_bf16 v[240:243], v[204:207], v[72:79], v25\n"
    "\tv_smfmac_f32_16x16x64_bf16 v[240:243], v[204:207], v[80:87], v25\n"
    "\tv_smfmac_f32_16x16x64_bf16 v[240:243], v[204:207], v[88:95], v25\n"
    "\tv_smfmac_f32_16x16x64_bf16 v[240:243], v[204:207], v[96:103], v25\n"
    "\tv_smfmac_f32_16x16x64_bf16 v[240:243], v[204:207], v[104:111], v25\n"
    "\ts_nop 7\n"
    "\tv_add_f32 v24, v240, v241\n"
    "\ts_barrier\n"
    "L%=_mloop:\n"
    "\tv_mov_b64 v[240:241], 0\n"
    "\tv_mov_b64 v[242:243], 0\n"
    "\tv_mov_b64 v[244:245], 0\n"
    "\tv_mov_b64 v[246:247], 0\n"
    "\tv_mov_b64 v[248:249], 0\n"
    "\tv_mov_b64 v[250:251], 0\n"
    "\ts_barrier\n"
    "\ts_barrier\n"
    "\tds_read_b128 v[204:207], v21\n"
    "\tds_read_b128 v[176:179], v20 offset:0\n"
    "\tds_read_b128 v[180:183], v20 offset:128\n"
    "\tds_read_b128 v[184:187], v20 offset:256\n"
    "\tds_read_b128 v[188:191], v20 offset:384\n"
    "\tds_read_b128 v[192:195], v20 offset:512\n"
    "\tds_read_b128 v[196:199], v20 offset:640\n"
    "\tds_read_b128 v[200:203], v20 offset:768\n"
    "\ts_mov_b32 s56, 0x3f803f80\n"
    "\tv_mov_b32 v210, 0x3b124925\n"
    "\ts_waitcnt lgkmcnt(7)\n"
    "\tv_dot2_f32_bf16 v208, v204, s56, 0\n"
    "\tv_dot2_f32_bf16 v209, v204, v204, 0\n"
    "\tv_dot2_f32_bf16 v208, v205, s56, v208\n"
    "\tv_dot2_f32_bf16 v209, v205, v205, v209\n"
    "\tv_dot2_f32_bf16 v208, v206, s56, v208\n"
    "\tv_dot2_f32_bf16 v209, v206, v206, v209\n"
    "\tv_dot2_f32_bf16 v208, v207, s56, v208\n"
    "\tv_dot2_f32_bf16 v209, v207, v207, v209\n"
    "\ts_waitcnt lgkmcnt(6)\n"
    "\tv_smfmac_f32_16x16x64_bf16 v[240:243], v[176:179], v[56:63], v25\n"
    "\tv_add_f32_dpp v208, v208, v208 quad_perm:[1,0,3,2] row_mask:0xf bank_mask:0xf\n"
    "\tv_add_f32_dpp v209, v209, v209 quad_perm:[1,0,3,2] row_mask:0xf bank_mask:0xf\n"
    "\ts_waitcnt lgkmcnt(5)\n"
    "\tv_smfmac_f32_16x16x64_bf16 v[240:243], v[180:183], v[64:71], v25\n"
    "\tv_add_f32_dpp v208, v208, v208 quad_perm:[2,3,0,1] row_mask:0xf bank_mask:0xf\n"
    "\tv_add_f32_dpp v209, v209, v209 quad_perm:[2,3,0,1] row_mask:0xf bank_mask:0xf\n"
    "\ts_waitcnt lgkmcnt(4)\n"
    "\tv_smfmac_f32_16x16x64_bf16 v[240:243], v[184:187], v[72:79], v25\n"
    "\tv_add_f32_dpp v208, v208, v208 row_half_mirror row_mask:0xf bank_mask:0xf\n"
    "\tv_add_f32_dpp v209, v209, v209 row_half_mirror row_mask:0xf bank_mask:0xf\n"
    "\ts_waitcnt lgkmcnt(3)\n"
    "\tv_smfmac_f32_16x16x64_bf16 v[240:243], v[188:191], v[80:87], v25\n"
    "\tv_add_f32_dpp v208, v208, v208 row_mirror row_mask:0xf bank_mask:0xf\n"
    "\tv_add_f32_dpp v209, v209, v209 row_mirror row_mask:0xf bank_mask:0xf\n"
    "\ts_waitcnt lgkmcnt(2)\n"
    "\tv_smfmac_f32_16x16x64_bf16 v[240:243], v[192:195], v[88:95], v25\n"
    "\tv_add_f32_dpp v208, v208, v208 row_bcast:15 row_mask:0xa bank_mask:0xf\n"
    "\tv_add_f32_dpp v209, v209, v209 row_bcast:15 row_mask:0xa bank_mask:0xf\n"
    "\ts_waitcnt lgkmcnt(1)\n"
    "\tv_smfmac_f32_16x16x64_bf16 v[240:243], v[196:199], v[96:103], v25\n"
    "\tv_add_f32_dpp v208, v208, v208 row_bcast:31 row_mask:0xc bank_mask:0xf\n"
    "\tv_add_f32_dpp v209, v209, v209 row_bcast:31 row_mask:0xc bank_mask:0xf\n"
    "\ts_waitcnt lgkmcnt(0)\n"
    "\tv_smfmac_f32_16x16x64_bf16 v[240:243], v[200:203], v[104:111], v25\n"
    "\tv_readlane_b32 s58, v208, 63\n"
    "\tv_readlane_b32 s59, v209, 63\n"
    "\ts_nop 0\n"
    "\tv_mul_f32 v204, s58, v210\n"
    "\tv_mul_f32 v205, s59, v210\n"
    "\tv_fma_f32 v205, -v204, v204, v205\n"
    "\tv_max_f32 v205, 0, v205\n"
    "\tv_add_f32 v205, 0x3727c5ac, v205\n"
    "\tv_rsq_f32 v205, v205\n"
    "\tv_add_f32 v210, v240, v241\n"
    "\tv_fma_f32 v210, -v204, v24, v210\n"
    "\tv_mul_f32 v210, v210, v205\n"
    "\tv_cvt_pk_bf16_f32 v210, v210, v210\n"
    "\tv_lshlrev_b32 v210, 16, v210\n"
    "\tv_mul_f32 v211, 0xbfb8aa3b, v210\n"
    "\tv_exp_f32 v211, v211\n"
    "\ts_nop 0\n"
    "\tv_add_f32 v211, 1.0, v211\n"
    "\tv_rcp_f32 v211, v211\n"
    "\ts_nop 0\n"
    "\tv_mul_f32 v210, v210, v211\n"
    "\tv_cvt_pk_bf16_f32 v210, v210, v210\n"
    "\tds_write_b16 v22, v210\n"
    "\ts_waitcnt lgkmcnt(0)\n"
    "\ts_barrier\n"
    "\tds_read_b128 v[176:179], v26\n"
    "\tds_read_b128 v[180:183], v27\n"
    "\tds_read_b128 v[184:187], v28\n"
    "\tds_read_b128 v[188:191], v29\n"
    "\ts_waitcnt lgkmcnt(3)\n"
    "\tv_smfmac_f32_16x16x64_bf16 v[244:247], v[176:179], v[112:119], v25\n"
    "\tv_smfmac_f32_16x16x64_bf16 v[248:251], v[176:179], v[120:127], v25\n"
    "\ts_waitcnt lgkmcnt(2)\n"
    "\tv_smfmac_f32_16x16x64_bf16 v[244:247], v[180:183], v[128:135], v25\n"
    "\tv_smfmac_f32_16x16x64_bf16 v[248:251], v[180:183], v[136:143], v25\n"
    "\ts_waitcnt lgkmcnt(1)\n"
    "\tv_smfmac_f32_16x16x64_bf16 v[244:247], v[184:187], v[144:151], v25\n"
    "\tv_smfmac_f32_16x16x64_bf16 v[248:251], v[184:187], v[152:159], v25\n"
    "\ts_waitcnt lgkmcnt(0)\n"
    "\tv_smfmac_f32_16x16x64_bf16 v[244:247], v[188:191], v[160:167], v25\n"
    "\tv_smfmac_f32_16x16x64_bf16 v[248:251], v[188:191], v[168:175], v25\n"
    "\ts_nop 6\n"
    "\tv_add_f32 v210, v244, v245\n"
    "\tv_add_f32 v211, v248, v249\n"
    "\tv_cvt_pk_bf16_f32 v210, v210, v211\n"
    "\tbuffer_store_dword v210, v23, s[36:39], s50 offen sc1\n"
    "\tbuffer_store_dword v210, v23, s[36:39], s79 offen sc1\n"
    "\ts_cmp_lg_u32 s52, 0\n"
    "\ts_cbranch_scc1 L%=_exit\n"
    "\ts_mov_b32 s52, 1\n"
    "\ts_mov_b32 s50, 655360\n"
    "\ts_mov_b32 s79, 663552\n"
    "\ts_barrier\n"
    "\ts_barrier\n"
    "\ts_branch L%=_mloop\n"
    "L%=_svc:\n"
    "\ts_cmp_ge_u32 s27, 14\n"
    "\ts_cselect_b32 s76, 1, 0\n"
    "\ts_mul_i32 s77, s76, 14\n"
    "\ts_sub_u32 s77, s27, s77\n"
    "\ts_cmp_eq_u32 s48, 3\n"
    "\ts_cbranch_scc1 L%=_s_q\n"
    "\ts_getreg_b32 s78, hwreg(HW_REG_XCC_ID, 2, 1)\n"
    "\ts_cmp_lg_u32 s48, 0\n"
    "\ts_cbranch_scc1 L%=_s_k\n"
    "\tv_mov_b64 v[64:65], 0\n"
    "\tv_mov_b64 v[66:67], 0\n"
    "\tv_lshlrev_b32 v60, 4, v2\n"
    "\tv_add_u32 v60, 161024, v60\n"
    "\ts_mov_b64 exec, 0xffff\n"
    "\tds_write_b128 v60, v[64:67]\n"
    "\ts_mov_b64 exec, -1\n"
    "\ts_cmp_ge_u32 s27, 28\n"
    "\ts_cbranch_scc1 L%=_s_pr\n"
    "\tv_xor_b32 v3, v6, v5\n"
    "\tv_lshlrev_b32 v3, 4, v3\n"
    "\tv_lshl_or_b32 v8, v5, 9, v3\n"
    "\tv_xor_b32 v9, 32, v8\n"
    "\tv_xor_b32 v10, 64, v8\n"
    "\tv_xor_b32 v11, 96, v8\n"
    "\tv_xor_b32 v12, 128, v8\n"
    "\tv_xor_b32 v13, 160, v8\n"
    "\tv_xor_b32 v14, 192, v8\n"
    "\tv_xor_b32 v15, 224, v8\n"
    "\tv_lshlrev_b32 v3, 1, v5\n"
    "\tv_xor_b32 v3, v6, v3\n"
    "\tv_lshlrev_b32 v3, 4, v3\n"
    "\tv_lshl_or_b32 v16, v5, 9, v3\n"
    "\tv_xor_b32 v17, 64, v16\n"
    "\tv_xor_b32 v18, 128, v16\n"
    "\tv_xor_b32 v19, 192, v16\n"
    "\ts_mul_i32 s47, s53, 0x9e3779b1\n"
    "\ts_lshr_b32 s47, s47, 16\n"
    "\ts_mul_i32 s44, s47, 9\n"
    "\ts_lshr_b32 s44, s44, 16\n"
    "\ts_lshl_b32 s44, s44, 13\n"
    "\ts_waitcnt lgkmcnt(0)\n"
    "\ts_mov_b32 s74, s64\n"
    "\ts_lshl_b32 s56, s26, 25\n"
    "\ts_add_u32 s56, s56, 31457280\n"
    "\ts_add_u32 s28, s10, s56\n"
    "\ts_addc_u32 s29, s11, 0\n"
    "\ts_and_b32 s29, s29, 0xffff\n"
    "\ts_mov_b32 s30, s4\n"
    "\ts_mov_b32 s31, 0x20000\n"
    "\ts_barrier\n"
    "\ts_mov_b32 s45, 3\n"
    "L%=_ld7:\n"
    "\ts_add_u32 m0, s44, 0\n"
    "\ts_add_u32 s46, s44, s49\n"
    "\tbuffer_load_dwordx4 v8, s[28:31], s46 offen offset:0 nt lds\n"
    "\ts_waitcnt vmcnt(24)\n"
    "\tbuffer_load_dwordx4 v9, s[28:31], s46 offen offset:1024 nt lds\n"
    "\ts_waitcnt vmcnt(24)\n"
    "\tbuffer_load_dwordx4 v10, s[28:31], s46 offen offset:2048 nt lds\n"
    "\ts_waitcnt vmcnt(24)\n"
    "\tbuffer_load_dwordx4 v11, s[28:31], s46 offen offset:3072 nt lds\n"
    "\ts_waitcnt vmcnt(24)\n"
    "\ts_add_u32 m0, m0, 4096\n"
    "\ts_add_u32 s46, s46, 4096\n"
    "\tbuffer_load_dwordx4 v12, s[28:31], s46 offen offset:0 nt lds\n"
    "\ts_waitcnt vmcnt(24)\n"
    "\tbuffer_load_dwordx4 v13, s[28:31], s46 offen offset:1024 nt lds\n"
    "\ts_waitcnt vmcnt(24)\n"
    "\tbuffer_load_dwordx4 v14, s[28:31], s46 offen offset:2048 nt lds\n"
    "\ts_waitcnt vmcnt(24)\n"
    "\tbuffer_load_dwordx4 v15, s[28:31], s46 offen offset:3072 nt lds\n"
    "\ts_waitcnt vmcnt(24)\n"
    "\ts_add_u32 s44, s44, 8192\n"
    "\ts_cmp_eq_u32 s44, 73728\n"
    "\ts_cselect_b32 s44, 0, s44\n"
    "\ts_sub_u32 s45, s45, 1\n"
    "\ts_cmp_lg_u32 s45, 0\n"
    "\ts_cbranch_scc1 L%=_ld7\n"
    "\ts_lshl_b32 s56, s26, 25\n"
    "\ts_add_u32 s56, s56, 31457280\n"
    "\ts_add_u32 s32, s12, s56\n"
    "\ts_addc_u32 s33, s13, 0\n"
    "\ts_and_b32 s33, s33, 0xffff\n"
    "\ts_mov_b32 s34, s4\n"
    "\ts_mov_b32 s35, 0x20000\n"
    "\ts_add_u32 s24, s24, 2097151\n"
    "\ts_addc_u32 s25, s25, 0\n"
    "\ts_and_b32 s24, s24, 4292870144\n"
    "\ts_mov_b32 s36, s24\n"
    "\ts_and_b32 s37, s25, 0xffff\n"
    "\ts_mov_b32 s38, 2097152\n"
    "\ts_mov_b32 s39, 0x20000\n"
    "\ts_mov_b32 s40, s16\n"
    "\ts_and_b32 s41, s17, 0xffff\n"
    "\ts_mov_b32 s42, 3670016\n"
    "\ts_mov_b32 s43, 0x20000\n"
    "\tv_lshrrev_b32 v3, 2, v1\n"
    "\tv_mul_u32_u24 v153, 896, v3\n"
    "\tv_bfe_u32 v3, v1, 0, 2\n"
    "\tv_lshlrev_b32 v35, 4, v3\n"
    "\tv_lshl_add_u32 v35, v153, 0, v35\n"
    "\ts_lshl_b32 s55, s26, 1\n"
    "\ts_add_u32 s55, s55, s76\n"
    "\ts_lshl_b32 s55, s55, 0\n"
    "\ts_mul_i32 s55, s55, 229376\n"
    "\ts_lshl_b32 s57, s77, 6\n"
    "\ts_add_u32 s55, s55, s57\n"
    "\tv_and_b32 v3, 1, v1\n"
    "\tv_lshlrev_b32 v3, 4, v3\n"
    "\tv_mov_b32 v7, 0xffff\n"
    "\tv_lshlrev_b32 v7, v3, v7\n"
    "\tv_lshrrev_b32 v3, 2, v2\n"
    "\tv_cmp_eq_u32 vcc, 0, v3\n"
    "\ts_nop 1\n"
    "\tv_cndmask_b32 v44, 0, v7, vcc\n"
    "\tv_and_b32 v48, 0x3f803f80, v44\n"
    "\tv_cmp_eq_u32 vcc, 1, v3\n"
    "\ts_nop 1\n"
    "\tv_cndmask_b32 v45, 0, v7, vcc\n"
    "\tv_and_b32 v49, 0x3f803f80, v45\n"
    "\tv_cmp_eq_u32 vcc, 2, v3\n"
    "\ts_nop 1\n"
    "\tv_cndmask_b32 v46, 0, v7, vcc\n"
    "\tv_and_b32 v50, 0x3f803f80, v46\n"
    "\tv_cmp_eq_u32 vcc, 3, v3\n"
    "\ts_nop 1\n"
    "\tv_cndmask_b32 v47, 0, v7, vcc\n"
    "\tv_and_b32 v51, 0x3f803f80, v47\n"
    "\ts_lshl_b32 s58, s48, 8\n"
    "\ts_add_u32 s57, s58, 157696\n"
    "\tv_lshrrev_b32 v3, 1, v2\n"
    "\tv_lshlrev_b32 v24, 5, v3\n"
    "\tv_bfe_u32 v3, v4, 0, 1\n"
    "\tv_lshl_add_u32 v24, v3, 4, v24\n"
    "\tv_bfe_u32 v3, v2, 0, 1\n"
    "\tv_lshl_add_u32 v24, v3, 3, v24\n"
    "\tv_lshl_add_u32 v24, v5, 2, v24\n"
    "\tv_add_u32 v24, s57, v24\n"
    "\ts_lshl_b32 s56, s48, 8\n"
    "\ts_cmp_eq_u32 s48, 3\n"
    "\ts_cselect_b32 s57, 8, 16\n"
    "\tv_lshlrev_b32 v26, 10, v4\n"
    "\tv_lshl_add_u32 v26, v2, 4, v26\n"
    "\tv_add_u32 v26, s56, v26\n"
    "\tv_mov_b32 v7, 1073741824\n"
    "\tv_cmp_gt_u32 vcc, s57, v2\n"
    "\ts_nop 1\n"
    "\tv_cndmask_b32 v26, v7, v26, vcc\n"
    "\tv_add_u32 v27, 4096, v26\n"
    "\tv_mov_b32 v7, 1073741824\n"
    "\tv_cmp_gt_u32 vcc, 16, v1\n"
    "\ts_nop 1\n"
    "\tv_cndmask_b32 v28, v7, v27, vcc\n"
    "\tv_lshlrev_b32 v32, 5, v4\n"
    "\tv_bfe_u32 v3, v1, 0, 1\n"
    "\tv_lshl_add_u32 v32, v3, 4, v32\n"
    "\tv_add_u32 v32, 154880, v32\n"
    "\ts_lshl_b32 s57, s26, 1\n"
    "\ts_add_u32 s57, s57, s76\n"
    "\ts_lshl_b32 s57, s57, 10\n"
    "\ts_lshr_b32 s61, s57, 13\n"
    "\ts_lshl_b32 s61, s61, 13\n"
    "\ts_add_u32 s57, s57, s61\n"
    "\ts_add_u32 s57, s57, 557056\n"
    "\ts_lshl_b32 s59, s77, 6\n"
    "\ts_add_u32 s57, s57, s59\n"
    "\tv_lshlrev_b32 v33, 2, v1\n"
    "\tv_add_u32 v33, s57, v33\n"
    "\tv_mov_b32 v7, 1073741824\n"
    "\tv_cmp_gt_u32 vcc, 16, v1\n"
    "\ts_nop 1\n"
    "\tv_cndmask_b32 v33, v7, v33, vcc\n"
    "\tv_lshrrev_b32 v3, 1, v2\n"
    "\tv_lshlrev_b32 v37, 5, v3\n"
    "\tv_bfe_u32 v3, v4, 0, 1\n"
    "\tv_lshl_add_u32 v37, v3, 4, v37\n"
    "\tv_bfe_u32 v3, v2, 0, 1\n"
    "\tv_lshl_add_u32 v37, v3, 3, v37\n"
    "\tv_lshl_add_u32 v37, v5, 2, v37\n"
    "\tv_add_u32 v37, 158720, v37\n"
    "\ts_lshl_b32 s57, s26, 12\n"
    "\ts_lshr_b32 s61, s57, 13\n"
    "\ts_lshl_b32 s61, s61, 13\n"
    "\ts_add_u32 s57, s57, s61\n"
    "\ts_add_u32 s57, s57, 1310720\n"
    "\tv_bfe_u32 v3, v2, 3, 1\n"
    "\tv_lshlrev_b32 v36, 9, v3\n"
    "\tv_lshl_add_u32 v36, v4, 7, v36\n"
    "\tv_bfe_u32 v3, v2, 0, 3\n"
    "\tv_lshl_add_u32 v36, v3, 4, v36\n"
    "\tv_add_u32 v36, s57, v36\n"
    "\tv_and_b32 v3, 1, v1\n"
    "\tv_mul_u32_u24 v3, 0xaaaa, v3\n"
    "\tv_add_u32 v22, 0x4444, v3\n"
    "\tv_bfe_u32 v3, v1, 2, 2\n"
    "\tv_lshlrev_b32 v29, 10, v3\n"
    "\tv_lshl_add_u32 v29, v4, 7, v29\n"
    "\tv_bfe_u32 v3, v1, 0, 2\n"
    "\tv_lshl_add_u32 v29, v3, 3, v29\n"
    "\ts_mov_b32 s45, 3\n"
    "L%=_ld8:\n"
    "\ts_add_u32 m0, s44, 0\n"
    "\ts_add_u32 s46, s44, s49\n"
    "\tbuffer_load_dwordx4 v8, s[28:31], s46 offen offset:0 nt lds\n"
    "\ts_waitcnt vmcnt(24)\n"
    "\tbuffer_load_dwordx4 v9, s[28:31], s46 offen offset:1024 nt lds\n"
    "\ts_waitcnt vmcnt(24)\n"
    "\tbuffer_load_dwordx4 v10, s[28:31], s46 offen offset:2048 nt lds\n"
    "\ts_waitcnt vmcnt(24)\n"
    "\tbuffer_load_dwordx4 v11, s[28:31], s46 offen offset:3072 nt lds\n"
    "\ts_waitcnt vmcnt(24)\n"
    "\ts_add_u32 m0, m0, 4096\n"
    "\ts_add_u32 s46, s46, 4096\n"
    "\tbuffer_load_dwordx4 v12, s[28:31], s46 offen offset:0 nt lds\n"
    "\ts_waitcnt vmcnt(24)\n"
    "\tbuffer_load_dwordx4 v13, s[28:31], s46 offen offset:1024 nt lds\n"
    "\ts_waitcnt vmcnt(24)\n"
    "\tbuffer_load_dwordx4 v14, s[28:31], s46 offen offset:2048 nt lds\n"
    "\ts_waitcnt vmcnt(24)\n"
    "\tbuffer_load_dwordx4 v15, s[28:31], s46 offen offset:3072 nt lds\n"
    "\ts_waitcnt vmcnt(24)\n"
    "\ts_add_u32 s44, s44, 8192\n"
    "\ts_cmp_eq_u32 s44, 73728\n"
    "\ts_cselect_b32 s44, 0, s44\n"
    "\ts_sub_u32 s45, s45, 1\n"
    "\ts_cmp_lg_u32 s45, 0\n"
    "\ts_cbranch_scc1 L%=_ld8\n"
    "\ts_mov_b32 m0, 75776\n"
    "\ts_nop 0\n"
    "\tbuffer_load_dwordx4 v35, s[40:43], s55 offen nt lds\n"
    "\ts_waitcnt vmcnt(24)\n"
    "\ts_add_u32 m0, m0, 1024\n"
    "\ts_add_u32 s55, s55, 14336\n"
    "\tbuffer_load_dwordx4 v35, s[40:43], s55 offen nt lds\n"
    "\ts_waitcnt vmcnt(24)\n"
    "\ts_add_u32 m0, m0, 1024\n"
    "\ts_add_u32 s55, s55, 14336\n"
    "\tbuffer_load_dwordx4 v35, s[40:43], s55 offen nt lds\n"
    "\ts_waitcnt vmcnt(24)\n"
    "\ts_add_u32 m0, m0, 1024\n"
    "\ts_add_u32 s55, s55, 14336\n"
    "\tbuffer_load_dwordx4 v35, s[40:43], s55 offen nt lds\n"
    "\ts_waitcnt vmcnt(24)\n"
    "\ts_add_u32 m0, m0, 1024\n"
    "\ts_add_u32 s55, s55, 14336\n"
    "\tbuffer_load_dwordx4 v35, s[40:43], s55 offen nt lds\n"
    "\ts_waitcnt vmcnt(24)\n"
    "\ts_add_u32 m0, m0, 1024\n"
    "\ts_add_u32 s55, s55, 14336\n"
    "\tbuffer_load_dwordx4 v35, s[40:43], s55 offen nt lds\n"
    "\ts_waitcnt vmcnt(24)\n"
    "\ts_add_u32 m0, m0, 1024\n"
    "\ts_add_u32 s55, s55, 14336\n"
    "\tbuffer_load_dwordx4 v35, s[40:43], s55 offen nt lds\n"
    "\ts_waitcnt vmcnt(24)\n"
    "\ts_add_u32 m0, m0, 1024\n"
    "\ts_add_u32 s55, s55, 14336\n"
    "\tbuffer_load_dwordx4 v35, s[40:43], s55 offen nt lds\n"
    "\ts_waitcnt vmcnt(24)\n"
    "\ts_add_u32 m0, m0, 1024\n"
    "\ts_add_u32 s55, s55, 14336\n"
    "\tbuffer_load_dwordx4 v35, s[40:43], s55 offen nt lds\n"
    "\ts_waitcnt vmcnt(24)\n"
    "\ts_add_u32 m0, m0, 1024\n"
    "\ts_add_u32 s55, s55, 14336\n"
    "\tbuffer_load_dwordx4 v35, s[40:43], s55 offen nt lds\n"
    "\ts_waitcnt vmcnt(24)\n"
    "\ts_add_u32 m0, m0, 1024\n"
    "\ts_add_u32 s55, s55, 14336\n"
    "\tbuffer_load_dwordx4 v35, s[40:43], s55 offen nt lds\n"
    "\ts_waitcnt vmcnt(24)\n"
    "\ts_add_u32 m0, m0, 1024\n"
    "\ts_add_u32 s55, s55, 14336\n"
    "\tbuffer_load_dwordx4 v35, s[40:43], s55 offen nt lds\n"
    "\ts_waitcnt vmcnt(24)\n"
    "\ts_add_u32 m0, m0, 1024\n"
    "\ts_add_u32 s55, s55, 14336\n"
    "\tbuffer_load_dwordx4 v35, s[40:43], s55 offen nt lds\n"
    "\ts_waitcnt vmcnt(24)\n"
    "\ts_add_u32 m0, m0, 1024\n"
    "\ts_add_u32 s55, s55, 14336\n"
    "\tbuffer_load_dwordx4 v35, s[40:43], s55 offen nt lds\n"
    "\ts_waitcnt vmcnt(24)\n"
    "\ts_add_u32 m0, m0, 1024\n"
    "\ts_add_u32 s55, s55, 14336\n"
    "\tbuffer_load_dwordx4 v35, s[40:43], s55 offen nt lds\n"
    "\ts_waitcnt vmcnt(24)\n"
    "\ts_add_u32 m0, m0, 1024\n"
    "\ts_add_u32 s55, s55, 14336\n"
    "\tbuffer_load_dwordx4 v35, s[40:43], s55 offen nt lds\n"
    "\ts_waitcnt vmcnt(24)\n"
    "\ts_mov_b32 s45, 3\n"
    "L%=_ld9:\n"
    "\ts_add_u32 m0, s44, 0\n"
    "\ts_add_u32 s46, s44, s49\n"
    "\tbuffer_load_dwordx4 v8, s[28:31], s46 offen offset:0 nt lds\n"
    "\ts_waitcnt vmcnt(24)\n"
    "\tbuffer_load_dwordx4 v9, s[28:31], s46 offen offset:1024 nt lds\n"
    "\ts_waitcnt vmcnt(24)\n"
    "\tbuffer_load_dwordx4 v10, s[28:31], s46 offen offset:2048 nt lds\n"
    "\ts_waitcnt vmcnt(24)\n"
    "\tbuffer_load_dwordx4 v11, s[28:31], s46 offen offset:3072 nt lds\n"
    "\ts_waitcnt vmcnt(24)\n"
    "\ts_add_u32 m0, m0, 4096\n"
    "\ts_add_u32 s46, s46, 4096\n"
    "\tbuffer_load_dwordx4 v12, s[28:31], s46 offen offset:0 nt lds\n"
    "\ts_waitcnt vmcnt(24)\n"
    "\tbuffer_load_dwordx4 v13, s[28:31], s46 offen offset:1024 nt lds\n"
    "\ts_waitcnt vmcnt(24)\n"
    "\tbuffer_load_dwordx4 v14, s[28:31], s46 offen offset:2048 nt lds\n"
    "\ts_waitcnt vmcnt(24)\n"
    "\tbuffer_load_dwordx4 v15, s[28:31], s46 offen offset:3072 nt lds\n"
    "\ts_waitcnt vmcnt(24)\n"
    "\ts_add_u32 s44, s44, 8192\n"
    "\ts_cmp_eq_u32 s44, 73728\n"
    "\ts_cselect_b32 s44, 0, s44\n"
    "\ts_sub_u32 s45, s45, 1\n"
    "\ts_cmp_lg_u32 s45, 0\n"
    "\ts_cbranch_scc1 L%=_ld9\n"
    "\ts_add_u32 s46, s49, 73728\n"
    "\ts_lshr_b32 s44, s47, 3\n"
    "\ts_and_b32 s44, s44, 7\n"
    "\ts_lshl_b32 s44, s44, 13\n"
    "\ts_mov_b32 m0, 73728\n"
    "\ts_nop 0\n"
    "\tbuffer_load_dwordx4 v8, s[28:31], s46 offen offset:0 nt lds\n"
    "\ts_waitcnt vmcnt(24)\n"
    "\tbuffer_load_dwordx4 v9, s[28:31], s46 offen offset:1024 nt lds\n"
    "\ts_waitcnt vmcnt(24)\n"
    "\tv_add_u32 v29, 75776, v29\n"
    "\tds_read_b64_tr_b16 v[56:57], v29 offset:0\n"
    "\tds_read_b64_tr_b16 v[58:59], v29 offset:64\n"
    "\tds_read_b64_tr_b16 v[60:61], v29 offset:512\n"
    "\tds_read_b64_tr_b16 v[62:63], v29 offset:576\n"
    "\tds_read_b64_tr_b16 v[64:65], v29 offset:4096\n"
    "\tds_read_b64_tr_b16 v[66:67], v29 offset:4160\n"
    "\tds_read_b64_tr_b16 v[68:69], v29 offset:4608\n"
    "\tds_read_b64_tr_b16 v[70:71], v29 offset:4672\n"
    "\ts_waitcnt lgkmcnt(4)\n"
    "\tds_read_b64_tr_b16 v[72:73], v29 offset:8192\n"
    "\tds_read_b64_tr_b16 v[74:75], v29 offset:8256\n"
    "\tds_read_b64_tr_b16 v[76:77], v29 offset:8704\n"
    "\tds_read_b64_tr_b16 v[78:79], v29 offset:8768\n"
    "\tds_read_b64_tr_b16 v[80:81], v29 offset:12288\n"
    "\tds_read_b64_tr_b16 v[82:83], v29 offset:12352\n"
    "\tds_read_b64_tr_b16 v[84:85], v29 offset:12800\n"
    "\tds_read_b64_tr_b16 v[86:87], v29 offset:12864\n"
    "\ts_waitcnt lgkmcnt(4)\n"
    "\tds_read_b64_tr_b16 v[88:89], v29 offset:32\n"
    "\tds_read_b64_tr_b16 v[90:91], v29 offset:96\n"
    "\tds_read_b64_tr_b16 v[92:93], v29 offset:544\n"
    "\tds_read_b64_tr_b16 v[94:95], v29 offset:608\n"
    "\tds_read_b64_tr_b16 v[96:97], v29 offset:4128\n"
    "\tds_read_b64_tr_b16 v[98:99], v29 offset:4192\n"
    "\tds_read_b64_tr_b16 v[100:101], v29 offset:4640\n"
    "\tds_read_b64_tr_b16 v[102:103], v29 offset:4704\n"
    "\ts_waitcnt lgkmcnt(4)\n"
    "\tds_read_b64_tr_b16 v[104:105], v29 offset:8224\n"
    "\tds_read_b64_tr_b16 v[106:107], v29 offset:8288\n"
    "\tds_read_b64_tr_b16 v[108:109], v29 offset:8736\n"
    "\tds_read_b64_tr_b16 v[110:111], v29 offset:8800\n"
    "\tds_read_b64_tr_b16 v[112:113], v29 offset:12320\n"
    "\tds_read_b64_tr_b16 v[114:115], v29 offset:12384\n"
    "\tds_read_b64_tr_b16 v[116:117], v29 offset:12832\n"
    "\tds_read_b64_tr_b16 v[118:119], v29 offset:12896\n"
    "\ts_waitcnt lgkmcnt(4)\n"
    "\ts_waitcnt lgkmcnt(0)\n"
    "\ts_mov_b32 s45, 3\n"
    "L%=_ld10:\n"
    "\ts_add_u32 m0, s44, 75776\n"
    "\ts_add_u32 s46, s44, s49\n"
    "\tbuffer_load_dwordx4 v16, s[32:35], s46 offen offset:0 nt lds\n"
    "\ts_waitcnt vmcnt(24)\n"
    "\tbuffer_load_dwordx4 v17, s[32:35], s46 offen offset:1024 nt lds\n"
    "\ts_waitcnt vmcnt(24)\n"
    "\tbuffer_load_dwordx4 v18, s[32:35], s46 offen offset:2048 nt lds\n"
    "\ts_waitcnt vmcnt(24)\n"
    "\tbuffer_load_dwordx4 v19, s[32:35], s46 offen offset:3072 nt lds\n"
    "\ts_waitcnt vmcnt(24)\n"
    "\ts_add_u32 m0, m0, 4096\n"
    "\ts_add_u32 s46, s46, 4096\n"
    "\tbuffer_load_dwordx4 v16, s[32:35], s46 offen offset:0 nt lds\n"
    "\ts_waitcnt vmcnt(24)\n"
    "\tbuffer_load_dwordx4 v17, s[32:35], s46 offen offset:1024 nt lds\n"
    "\ts_waitcnt vmcnt(24)\n"
    "\tbuffer_load_dwordx4 v18, s[32:35], s46 offen offset:2048 nt lds\n"
    "\ts_waitcnt vmcnt(24)\n"
    "\tbuffer_load_dwordx4 v19, s[32:35], s46 offen offset:3072 nt lds\n"
    "\ts_waitcnt vmcnt(24)\n"
    "\ts_add_u32 s44, s44, 8192\n"
    "\ts_cmp_eq_u32 s44, 65536\n"
    "\ts_cselect_b32 s44, 0, s44\n"
    "\ts_sub_u32 s45, s45, 1\n"
    "\ts_cmp_lg_u32 s45, 0\n"
    "\ts_cbranch_scc1 L%=_ld10\n"
    "\tv_mov_b32 v3, 161040\n"
    "\tv_mov_b32 v7, 1\n"
    "\ts_mov_b64 exec, 1\n"
    "\tds_add_u32 v3, v7\n"
    "\ts_mov_b64 exec, -1\n"
    "\ts_mov_b32 s45, 5\n"
    "L%=_ld11:\n"
    "\ts_add_u32 m0, s44, 75776\n"
    "\ts_add_u32 s46, s44, s49\n"
    "\tbuffer_load_dwordx4 v16, s[32:35], s46 offen offset:0 nt lds\n"
    "\ts_waitcnt vmcnt(24)\n"
    "\tbuffer_load_dwordx4 v17, s[32:35], s46 offen offset:1024 nt lds\n"
    "\ts_waitcnt vmcnt(24)\n"
    "\tbuffer_load_dwordx4 v18, s[32:35], s46 offen offset:2048 nt lds\n"
    "\ts_waitcnt vmcnt(24)\n"
    "\tbuffer_load_dwordx4 v19, s[32:35], s46 offen offset:3072 nt lds\n"
    "\ts_waitcnt vmcnt(24)\n"
    "\ts_add_u32 m0, m0, 4096\n"
    "\ts_add_u32 s46, s46, 4096\n"
    "\tbuffer_load_dwordx4 v16, s[32:35], s46 offen offset:0 nt lds\n"
    "\ts_waitcnt vmcnt(24)\n"
    "\tbuffer_load_dwordx4 v17, s[32:35], s46 offen offset:1024 nt lds\n"
    "\ts_waitcnt vmcnt(24)\n"
    "\tbuffer_load_dwordx4 v18, s[32:35], s46 offen offset:2048 nt lds\n"
    "\ts_waitcnt vmcnt(24)\n"
    "\tbuffer_load_dwordx4 v19, s[32:35], s46 offen offset:3072 nt lds\n"
    "\ts_waitcnt vmcnt(24)\n"
    "\ts_add_u32 s44, s44, 8192\n"
    "\ts_cmp_eq_u32 s44, 65536\n"
    "\ts_cselect_b32 s44, 0, s44\n"
    "\ts_sub_u32 s45, s45, 1\n"
    "\ts_cmp_lg_u32 s45, 0\n"
    "\ts_cbranch_scc1 L%=_ld11\n"
    "\tv_mov_b32 v3, 161064\n"
    "L%=_fw12:\n"
    "\tds_read_b32 v7, v3\n"
    "\ts_waitcnt lgkmcnt(0)\n"
    "\tv_readfirstlane_b32 s61, v7\n"
    "\ts_nop 0\n"
    "\ts_cmp_lt_u32 s61, 1\n"
    "\ts_cbranch_scc1 L%=_fw12\n"
    "\tv_mov_b32 v3, 161048\n"
    "L%=_fw13:\n"
    "\tds_read_b32 v7, v3\n"
    "\ts_waitcnt lgkmcnt(0)\n"
    "\tv_readfirstlane_b32 s61, v7\n"
    "\ts_nop 0\n"
    "\ts_cmp_lt_u32 s61, 4\n"
    "\ts_cbranch_scc1 L%=_fw13\n"
    "\ts_add_u32 s46, s49, 65536\n"
    "\ts_mov_b32 m0, 141312\n"
    "\ts_nop 0\n"
    "\tbuffer_load_dwordx4 v16, s[32:35], s46 offen offset:0 nt lds\n"
    "\ts_waitcnt vmcnt(24)\n"
    "\tbuffer_load_dwordx4 v17, s[32:35], s46 offen offset:1024 nt lds\n"
    "\ts_waitcnt vmcnt(24)\n"
    "\tbuffer_load_dwordx4 v18, s[32:35], s46 offen offset:2048 nt lds\n"
    "\ts_waitcnt vmcnt(24)\n"
    "\tbuffer_load_dwordx4 v19, s[32:35], s46 offen offset:3072 nt lds\n"
    "\ts_waitcnt vmcnt(24)\n"
    "\ts_add_u32 m0, m0, 4096\n"
    "\ts_add_u32 s46, s46, 4096\n"
    "\tbuffer_load_dwordx4 v16, s[32:35], s46 offen offset:0 nt lds\n"
    "\ts_waitcnt vmcnt(24)\n"
    "\tbuffer_load_dwordx4 v17, s[32:35], s46 offen offset:1024 nt lds\n"
    "\ts_waitcnt vmcnt(24)\n"
    "\tbuffer_load_dwordx4 v18, s[32:35], s46 offen offset:2048 nt lds\n"
    "\ts_waitcnt vmcnt(24)\n"
    "\tbuffer_load_dwordx4 v19, s[32:35], s46 offen offset:3072 nt lds\n"
    "\ts_waitcnt vmcnt(24)\n"
    "\ts_add_u32 m0, m0, 4096\n"
    "\ts_add_u32 s46, s46, 4096\n"
    "\tbuffer_load_dwordx4 v16, s[32:35], s46 offen offset:0 nt lds\n"
    "\ts_waitcnt vmcnt(24)\n"
    "\tbuffer_load_dwordx4 v17, s[32:35], s46 offen offset:1024 nt lds\n"
    "\ts_waitcnt vmcnt(24)\n"
    "\ts_branch L%=_as_w4\n"
    "L%=_s_pr:\n"
    "\ts_lshl_b32 s56, s54, 9\n"
    "\ts_lshr_b32 s61, s56, 13\n"
    "\ts_lshl_b32 s61, s61, 13\n"
    "\ts_add_u32 s56, s56, s61\n"
    "\ts_add_u32 s56, s56, 1572864\n"
    "\tv_mov_b32 v60, s56\n"
    "\ts_and_b32 s57, s27, 1\n"
    "\ts_lshl_b32 s58, s57, 13\n"
    "\ts_xor_b32 s59, s58, 8192\n"
    "\ts_waitcnt lgkmcnt(0)\n"
    "\ts_mov_b32 s74, s64\n"
    "\ts_add_u32 s24, s24, 2097151\n"
    "\ts_addc_u32 s25, s25, 0\n"
    "\ts_and_b32 s24, s24, 4292870144\n"
    "\ts_mov_b32 s36, s24\n"
    "\ts_and_b32 s37, s25, 0xffff\n"
    "\ts_mov_b32 s38, 2097152\n"
    "\ts_mov_b32 s39, 0x20000\n"
    "\tbuffer_load_dword v61, v60, s[36:39], s58 offen sc1\n"
    "\tbuffer_load_dword v62, v60, s[36:39], s59 offen sc1\n"
    "\ts_waitcnt vmcnt(1)\n"
    "\ts_memtime s[62:63]\n"
    "\ts_waitcnt vmcnt(0)\n"
    "\ts_memtime s[64:65]\n"
    "\ts_barrier\n"
    "\ts_mov_b32 s61, 1560576\n"
    "\ts_load_dwordx2 s[70:71], s[24:25], s61\n"
    "\ts_waitcnt lgkmcnt(0)\n"
    "\ts_sub_u32 s56, s64, s62\n"
    "\ts_min_u32 s56, s56, 400\n"
    "\ts_sub_u32 s58, 0, s56\n"
    "\ts_cmp_eq_u32 s57, 0\n"
    "\ts_cselect_b32 s58, s58, s56\n"
    "\ts_add_u32 s70, s70, 0x7f4a7c15\n"
    "\ts_addc_u32 s71, s71, 0x9e3779b9\n"
    "\ts_and_b32 s72, s71, -2\n"
    "\ts_lshl_b32 s59, s54, 7\n"
    "\ts_add_u32 s59, s59, 1556480\n"
    "\tv_mov_b32 v60, s59\n"
    "\ts_sub_u32 s56, 0, s58\n"
    "\ts_cmp_eq_u32 s78, 0\n"
    "\ts_cselect_b32 s58, s58, s56\n"
    "\ts_xor_b32 s58, s58, s70\n"
    "\tv_mov_b32 v64, s70\n"
    "\tv_mov_b32 v65, s71\n"
    "\tv_mov_b32 v66, s58\n"
    "\tv_mov_b32 v67, s71\n"
    "\tv_mov_b32 v7, 1073741824\n"
    "\tv_cmp_eq_u32 vcc, 0, v1\n"
    "\ts_nop 1\n"
    "\tv_cndmask_b32 v60, v7, v60, vcc\n"
    "\tv_mov_b32 v3, 161044\n"
    "L%=_fw14:\n"
    "\ts_sleep 2\n"
    "\tds_read_b32 v7, v3\n"
    "\ts_waitcnt lgkmcnt(0)\n"
    "\tv_readfirstlane_b32 s61, v7\n"
    "\ts_nop 0\n"
    "\ts_cmp_lt_u32 s61, 2\n"
    "\ts_cbranch_scc1 L%=_fw14\n"
    "\tbuffer_store_dwordx4 v[64:67], v60, s[36:39], 0 offen sc1\n"
    "\ts_branch L%=_s_mid\n"
    "L%=_s_k:\n"
    "\ts_cmp_ge_u32 s27, 28\n"
    "\ts_cbranch_scc1 L%=_s_kb\n"
    "\ts_waitcnt lgkmcnt(0)\n"
    "\ts_mov_b32 s74, s64\n"
    "\ts_add_u32 s24, s24, 2097151\n"
    "\ts_addc_u32 s25, s25, 0\n"
    "\ts_and_b32 s24, s24, 4292870144\n"
    "\ts_mov_b32 s36, s24\n"
    "\ts_and_b32 s37, s25, 0xffff\n"
    "\ts_mov_b32 s38, 2097152\n"
    "\ts_mov_b32 s39, 0x20000\n"
    "\ts_mov_b32 s61, 1560576\n"
    "\ts_load_dwordx2 s[70:71], s[24:25], s61\n"
    "\ts_barrier\n"
    "\ts_waitcnt lgkmcnt(0)\n"
    "\tv_mov_b32 v3, 161044\n"
    "\tv_mov_b32 v7, 1\n"
    "\ts_mov_b64 exec, 1\n"
    "\tds_add_u32 v3, v7\n"
    "\ts_mov_b64 exec, -1\n"
    "\ts_add_u32 s70, s70, 0x7f4a7c15\n"
    "\ts_addc_u32 s71, s71, 0x9e3779b9\n"
    "\ts_and_b32 s72, s71, -2\n"
    "\ts_branch L%=_s_mid\n"
    "L%=_s_kb:\n"
    "\tv_mov_b32 v128, -1\n"
    "\tv_mov_b32 v129, -1\n"
    "\tv_mov_b32 v130, -1\n"
    "\tv_mov_b32 v131, -1\n"
    "\ts_sub_u32 s55, s27, 28\n"
    "\tv_lshlrev_b32 v124, 4, v1\n"
    "\ts_lshl_b32 s56, s26, 2\n"
    "\ts_add_u32 s56, s56, s55\n"
    "\ts_lshl_b32 s56, s56, 9\n"
    "\ts_lshr_b32 s61, s56, 13\n"
    "\ts_lshl_b32 s61, s61, 13\n"
    "\ts_add_u32 s56, s56, s61\n"
    "\ts_add_u32 s56, s56, 524288\n"
    "\tv_bfe_u32 v3, v1, 3, 1\n"
    "\tv_lshlrev_b32 v125, 0, v3\n"
    "\tv_mul_u32_u24 v125, 655360, v125\n"
    "\tv_bfe_u32 v3, v1, 0, 3\n"
    "\tv_lshlrev_b32 v126, 6, v3\n"
    "\tv_lshl_add_u32 v126, v125, 0, v126\n"
    "\tv_add_u32 v126, s56, v126\n"
    "\ts_lshl_b32 s56, s55, 12\n"
    "\ts_lshr_b32 s61, s56, 13\n"
    "\ts_lshl_b32 s61, s61, 13\n"
    "\ts_add_u32 s56, s56, s61\n"
    "\ts_add_u32 s56, s56, 1376256\n"
    "\ts_lshl_b32 s57, s26, 6\n"
    "\ts_add_u32 s56, s56, s57\n"
    "\tv_bfe_u32 v3, v1, 0, 3\n"
    "\tv_lshlrev_b32 v125, 9, v3\n"
    "\tv_add_u32 v125, s56, v125\n"
    "\tv_cmp_gt_u32 vcc, 16, v1\n"
    "\ts_nop 1\n"
    "\tv_cndmask_b32 v125, v125, v126, vcc\n"
    "\tv_mov_b32 v7, 1073741824\n"
    "\tv_cmp_gt_u32 vcc, 24, v1\n"
    "\ts_nop 1\n"
    "\tv_cndmask_b32 v125, v7, v125, vcc\n"
    "\ts_lshl_b32 s58, s54, 6\n"
    "\ts_add_u32 s58, s58, 1409024\n"
    "\tv_writelane_b32 v125, s58, 24\n"
    "\tv_mov_b32 v7, 1073741824\n"
    "\tv_cmp_gt_u32 vcc, 56, v1\n"
    "\ts_nop 1\n"
    "\tv_cndmask_b32 v126, v7, v124, vcc\n"
    "\ts_sub_u32 s75, s48, 1\n"
    "\ts_lshl_b32 s75, s75, 13\n"
    "\ts_waitcnt lgkmcnt(0)\n"
    "\ts_mov_b32 s74, s64\n"
    "\ts_add_u32 s24, s24, 2097151\n"
    "\ts_addc_u32 s25, s25, 0\n"
    "\ts_and_b32 s24, s24, 4292870144\n"
    "\ts_mov_b32 s36, s24\n"
    "\ts_and_b32 s37, s25, 0xffff\n"
    "\ts_mov_b32 s38, 2097152\n"
    "\ts_mov_b32 s39, 0x20000\n"
    "\ts_mov_b32 s61, 1560576\n"
    "\ts_load_dwordx2 s[70:71], s[24:25], s61\n"
    "\ts_barrier\n"
    "\tbuffer_store_dwordx4 v[128:131], v125, s[36:39], s75 offen sc1\n"
    "\ts_lshl_b32 s56, s26, 2\n"
    "\ts_add_u32 s56, s56, s55\n"
    "\ts_lshl_b32 s56, s56, 10\n"
    "\ts_lshr_b32 s61, s56, 13\n"
    "\ts_lshl_b32 s61, s61, 13\n"
    "\ts_add_u32 s56, s56, s61\n"
    "\ts_add_u32 s56, s56, 1310720\n"
    "\ts_add_u32 s56, s56, s75\n"
    "\tbuffer_store_dwordx4 v[128:131], v124, s[36:39], s56 offen offset:0 sc1\n"
    "\ts_waitcnt vmcnt(30)\n"
    "\ts_lshl_b32 s56, s54, 10\n"
    "\ts_lshr_b32 s61, s56, 13\n"
    "\ts_lshl_b32 s61, s61, 13\n"
    "\ts_add_u32 s56, s56, s61\n"
    "\ts_add_u32 s56, s56, 589824\n"
    "\ts_add_u32 s56, s56, s75\n"
    "\tbuffer_store_dwordx4 v[128:131], v126, s[36:39], s56 offen offset:0 sc1\n"
    "\ts_waitcnt vmcnt(30)\n"
    "\ts_lshl_b32 s56, s26, 1\n"
    "\ts_add_u32 s56, s56, s55\n"
    "\ts_lshl_b32 s56, s56, 10\n"
    "\ts_lshr_b32 s61, s56, 13\n"
    "\ts_lshl_b32 s61, s61, 13\n"
    "\ts_add_u32 s56, s56, s61\n"
    "\ts_add_u32 s56, s56, 557056\n"
    "\ts_add_u32 s56, s56, s75\n"
    "\ts_cmp_lt_u32 s55, 2\n"
    "\ts_cselect_b32 s56, s56, 1073741824\n"
    "\tbuffer_store_dwordx4 v[128:131], v126, s[36:39], s56 offen offset:0 sc1\n"
    "\ts_waitcnt vmcnt(30)\n"
    "\ts_lshl_b32 s56, s26, 3\n"
    "\ts_add_u32 s56, s56, s55\n"
    "\ts_lshl_b32 s56, s56, 12\n"
    "\ts_lshr_b32 s61, s56, 13\n"
    "\ts_lshl_b32 s61, s61, 13\n"
    "\ts_add_u32 s56, s56, s61\n"
    "\ts_add_u32 s56, s56, s75\n"
    "\ts_cmp_lt_u32 s55, 3\n"
    "\ts_cselect_b32 s57, s56, 1073741824\n"
    "\tbuffer_store_dwordx4 v[128:131], v124, s[36:39], s56 offen offset:0 sc1\n"
    "\ts_waitcnt vmcnt(30)\n"
    "\tbuffer_store_dwordx4 v[128:131], v124, s[36:39], s56 offen offset:1024 sc1\n"
    "\ts_waitcnt vmcnt(30)\n"
    "\tbuffer_store_dwordx4 v[128:131], v124, s[36:39], s57 offen offset:2048 sc1\n"
    "\ts_waitcnt vmcnt(30)\n"
    "\tbuffer_store_dwordx4 v[128:131], v124, s[36:39], s57 offen offset:3072 sc1\n"
    "\ts_waitcnt vmcnt(30)\n"
    "\ts_lshl_b32 s56, s26, 3\n"
    "\ts_add_u32 s56, s56, s55\n"
    "\ts_lshl_b32 s56, s56, 12\n"
    "\ts_lshr_b32 s61, s56, 13\n"
    "\ts_lshl_b32 s61, s61, 13\n"
    "\ts_add_u32 s56, s56, s61\n"
    "\ts_add_u32 s56, s56, 32768\n"
    "\ts_add_u32 s56, s56, s75\n"
    "\ts_cmp_lt_u32 s55, 3\n"
    "\ts_cselect_b32 s57, s56, 1073741824\n"
    "\tbuffer_store_dwordx4 v[128:131], v124, s[36:39], s56 offen offset:0 sc1\n"
    "\ts_waitcnt vmcnt(30)\n"
    "\tbuffer_store_dwordx4 v[128:131], v124, s[36:39], s56 offen offset:1024 sc1\n"
    "\ts_waitcnt vmcnt(30)\n"
    "\tbuffer_store_dwordx4 v[128:131], v124, s[36:39], s57 offen offset:2048 sc1\n"
    "\ts_waitcnt vmcnt(30)\n"
    "\tbuffer_store_dwordx4 v[128:131], v124, s[36:39], s57 offen offset:3072 sc1\n"
    "\ts_waitcnt vmcnt(30)\n"
    "\ts_lshl_b32 s56, s54, 10\n"
    "\ts_lshr_b32 s61, s56, 13\n"
    "\ts_lshl_b32 s61, s61, 13\n"
    "\ts_add_u32 s56, s56, s61\n"
    "\ts_add_u32 s56, s56, 1245184\n"
    "\ts_add_u32 s56, s56, s75\n"
    "\tbuffer_store_dwordx4 v[128:131], v126, s[36:39], s56 offen offset:0 sc1\n"
    "\ts_waitcnt vmcnt(30)\n"
    "\ts_lshl_b32 s56, s26, 1\n"
    "\ts_add_u32 s56, s56, s55\n"
    "\ts_lshl_b32 s56, s56, 10\n"
    "\ts_lshr_b32 s61, s56, 13\n"
    "\ts_lshl_b32 s61, s61, 13\n"
    "\ts_add_u32 s56, s56, s61\n"
    "\ts_add_u32 s56, s56, 1212416\n"
    "\ts_add_u32 s56, s56, s75\n"
    "\ts_cmp_lt_u32 s55, 2\n"
    "\ts_cselect_b32 s56, s56, 1073741824\n"
    "\tbuffer_store_dwordx4 v[128:131], v126, s[36:39], s56 offen offset:0 sc1\n"
    "\ts_waitcnt vmcnt(30)\n"
    "\ts_waitcnt vmcnt(0) lgkmcnt(0)\n"
    "\tv_mov_b32 v3, 161044\n"
    "\tv_mov_b32 v7, 1\n"
    "\ts_mov_b64 exec, 1\n"
    "\tds_add_u32 v3, v7\n"
    "\ts_mov_b64 exec, -1\n"
    "\ts_add_u32 s70, s70, 0x7f4a7c15\n"
    "\ts_addc_u32 s71, s71, 0x9e3779b9\n"
    "\ts_and_b32 s72, s71, -2\n"
    "\ts_branch L%=_s_mid\n"
    "L%=_s_q:\n"
    "\tv_lshlrev_b32 v7, 1, v5\n"
    "\tv_xor_b32 v122, v1, v7\n"
    "\tv_lshrrev_b32 v3, 3, v1\n"
    "\tv_lshlrev_b32 v120, 13, v3\n"
    "\tv_bfe_u32 v3, v122, 0, 3\n"
    "\tv_lshl_add_u32 v120, v3, 4, v120\n"
    "\tv_xor_b32 v122, 64, v120\n"
    "\tv_lshlrev_b32 v121, 2, v1\n"
    "\ts_and_b32 s57, s27, 3\n"
    "\ts_lshl_b32 s57, s57, 20\n"
    "\ts_lshl_b32 s58, s26, 10\n"
    "\ts_add_u32 s57, s57, s58\n"
    "\ts_lshr_b32 s58, s27, 2\n"
    "\ts_lshl_b32 s58, s58, 7\n"
    "\ts_add_u32 s57, s57, s58\n"
    "\ts_waitcnt lgkmcnt(0)\n"
    "\ts_mov_b32 s74, s64\n"
    "\ts_mov_b32 s40, s14\n"
    "\ts_and_b32 s41, s15, 0xffff\n"
    "\ts_mov_b32 s42, 3670016\n"
    "\ts_mov_b32 s43, 0x20000\n"
    "\ts_cmp_lt_u32 s27, 28\n"
    "\ts_cbranch_scc1 L%=_s_q1\n"
    "\ts_barrier\n"
    "L%=_s_q1:\n"
    "\ts_mov_b32 m0, 141312\n"
    "\ts_nop 0\n"
    "\tbuffer_load_dwordx4 v120, s[40:43], s57 offen nt lds\n"
    "\ts_add_u32 m0, m0, 1024\n"
    "\ts_add_u32 s57, s57, 65536\n"
    "\tbuffer_load_dwordx4 v120, s[40:43], s57 offen nt lds\n"
    "\ts_add_u32 m0, m0, 1024\n"
    "\ts_add_u32 s57, s57, 65536\n"
    "\tbuffer_load_dwordx4 v122, s[40:43], s57 offen nt lds\n"
    "\ts_add_u32 m0, m0, 1024\n"
    "\ts_add_u32 s57, s57, 65536\n"
    "\tbuffer_load_dwordx4 v122, s[40:43], s57 offen nt lds\n"
    "\ts_add_u32 m0, m0, 1024\n"
    "\ts_add_u32 s57, s57, 65536\n"
    "\tbuffer_load_dwordx4 v120, s[40:43], s57 offen nt lds\n"
    "\ts_add_u32 m0, m0, 1024\n"
    "\ts_add_u32 s57, s57, 65536\n"
    "\tbuffer_load_dwordx4 v120, s[40:43], s57 offen nt lds\n"
    "\ts_add_u32 m0, m0, 1024\n"
    "\ts_add_u32 s57, s57, 65536\n"
    "\tbuffer_load_dwordx4 v122, s[40:43], s57 offen nt lds\n"
    "\ts_add_u32 m0, m0, 1024\n"
    "\ts_add_u32 s57, s57, 65536\n"
    "\tbuffer_load_dwordx4 v122, s[40:43], s57 offen nt lds\n"
    "\ts_add_u32 m0, m0, 1024\n"
    "\ts_add_u32 s57, s57, 65536\n"
    "\tbuffer_load_dwordx4 v120, s[40:43], s57 offen nt lds\n"
    "\ts_add_u32 m0, m0, 1024\n"
    "\ts_add_u32 s57, s57, 65536\n"
    "\tbuffer_load_dwordx4 v120, s[40:43], s57 offen nt lds\n"
    "\ts_add_u32 m0, m0, 1024\n"
    "\ts_add_u32 s57, s57, 65536\n"
    "\tbuffer_load_dwordx4 v122, s[40:43], s57 offen nt lds\n"
    "\ts_add_u32 m0, m0, 1024\n"
    "\ts_add_u32 s57, s57, 65536\n"
    "\tbuffer_load_dwordx4 v122, s[40:43], s57 offen nt lds\n"
    "\ts_add_u32 m0, m0, 1024\n"
    "\ts_add_u32 s57, s57, 65536\n"
    "\tbuffer_load_dwordx4 v120, s[40:43], s57 offen nt lds\n"
    "\ts_add_u32 m0, m0, 1024\n"
    "\ts_add_u32 s57, s57, 65536\n"
    "\tbuffer_load_dwordx4 v120, s[40:43], s57 offen nt lds\n"
    "\ts_add_u32 m0, m0, 1024\n"
    "\ts_add_u32 s57, s57, 65536\n"
    "\tbuffer_load_dwordx4 v122, s[40:43], s57 offen nt lds\n"
    "\ts_add_u32 m0, m0, 1024\n"
    "\ts_add_u32 s57, s57, 65536\n"
    "\tbuffer_load_dwordx4 v122, s[40:43], s57 offen nt lds\n"
    "\ts_mov_b32 s44, s8\n"
    "\ts_and_b32 s45, s9, 0xffff\n"
    "\ts_mov_b32 s46, 896\n"
    "\ts_mov_b32 s47, 0x20000\n"
    "\ts_mov_b32 m0, 160000\n"
    "\ts_nop 0\n"
    "\tbuffer_load_dword v121, s[44:47], 0 offen offset:0 lds\n"
    "\tbuffer_load_dword v121, s[44:47], 0 offen offset:256 lds\n"
    "\tbuffer_load_dword v121, s[44:47], 0 offen offset:512 lds\n"
    "\tbuffer_load_dword v121, s[44:47], 0 offen offset:768 lds\n"
    "\ts_cmp_ge_u32 s27, 28\n"
    "\ts_cbranch_scc1 L%=_s_q2\n"
    "\ts_barrier\n"
    "L%=_s_q2:\n"
    "\ts_add_u32 s24, s24, 2097151\n"
    "\ts_addc_u32 s25, s25, 0\n"
    "\ts_and_b32 s24, s24, 4292870144\n"
    "\ts_mov_b32 s36, s24\n"
    "\ts_and_b32 s37, s25, 0xffff\n"
    "\ts_mov_b32 s38, 2097152\n"
    "\ts_mov_b32 s39, 0x20000\n"
    "\ts_mov_b32 s61, 1560576\n"
    "\ts_load_dwordx2 s[70:71], s[24:25], s61\n"
    "\tv_and_b32 v3, 1, v1\n"
    "\tv_mul_u32_u24 v3, 0xaaaa, v3\n"
    "\tv_add_u32 v22, 0x4444, v3\n"
    "\tv_lshlrev_b32 v21, 4, v1\n"
    "\tv_add_u32 v21, 157696, v21\n"
    "\ts_and_b32 s56, s27, 3\n"
    "\ts_lshl_b32 s56, s56, 8\n"
    "\ts_add_u32 s56, s56, 157696\n"
    "\tv_lshlrev_b32 v152, 5, v4\n"
    "\tv_bfe_u32 v3, v1, 0, 1\n"
    "\tv_lshl_add_u32 v152, v3, 4, v152\n"
    "\tv_add_u32 v152, s56, v152\n"
    "\tv_lshrrev_b32 v3, 2, v2\n"
    "\tv_mov_b32 v7, 161088\n"
    "\tv_cmp_eq_u32 vcc, 0, v3\n"
    "\ts_nop 1\n"
    "\tv_cndmask_b32 v40, v7, v152, vcc\n"
    "\tv_cmp_eq_u32 vcc, 1, v3\n"
    "\ts_nop 1\n"
    "\tv_cndmask_b32 v41, v7, v152, vcc\n"
    "\tv_cmp_eq_u32 vcc, 2, v3\n"
    "\ts_nop 1\n"
    "\tv_cndmask_b32 v42, v7, v152, vcc\n"
    "\tv_cmp_eq_u32 vcc, 3, v3\n"
    "\ts_nop 1\n"
    "\tv_cndmask_b32 v43, v7, v152, vcc\n"
    "\ts_lshl_b32 s56, s26, 5\n"
    "\ts_add_u32 s56, s56, s27\n"
    "\ts_lshl_b32 s56, s56, 7\n"
    "\ts_lshr_b32 s61, s56, 13\n"
    "\ts_lshl_b32 s61, s61, 13\n"
    "\ts_add_u32 s56, s56, s61\n"
    "\ts_add_u32 s56, s56, 1310720\n"
    "\tv_lshrrev_b32 v3, 1, v1\n"
    "\tv_lshlrev_b32 v34, 2, v3\n"
    "\tv_add_u32 v34, s56, v34\n"
    "\tv_and_b32 v152, 1, v1\n"
    "\tv_mov_b32 v7, 1073741824\n"
    "\tv_cmp_eq_u32 vcc, 0, v152\n"
    "\ts_nop 1\n"
    "\tv_cndmask_b32 v34, v7, v34, vcc\n"
    "\ts_lshl_b32 s56, s53, 8\n"
    "\ts_lshr_b32 s61, s56, 13\n"
    "\ts_lshl_b32 s61, s61, 13\n"
    "\ts_add_u32 s56, s56, s61\n"
    "\ts_add_u32 s56, s56, 1425408\n"
    "\tv_lshrrev_b32 v3, 1, v1\n"
    "\tv_lshlrev_b32 v176, 3, v3\n"
    "\tv_add_u32 v176, s56, v176\n"
    "\tv_mov_b32 v7, 1073741824\n"
    "\tv_cmp_eq_u32 vcc, 0, v152\n"
    "\ts_nop 1\n"
    "\tv_cndmask_b32 v176, v7, v176, vcc\n"
    "\tv_lshrrev_b32 v3, 2, v1\n"
    "\tv_lshlrev_b32 v177, 6, v3\n"
    "\tv_bfe_u32 v3, v1, 0, 2\n"
    "\tv_lshl_add_u32 v177, v3, 3, v177\n"
    "\tv_add_u32 v177, 160000, v177\n"
    "\tv_lshrrev_b32 v3, 1, v1\n"
    "\tv_lshlrev_b32 v178, 5, v3\n"
    "\tv_bfe_u32 v3, v1, 0, 1\n"
    "\tv_lshl_add_u32 v178, v3, 3, v178\n"
    "\tv_add_u32 v178, 157696, v178\n"
    "\tv_lshlrev_b32 v180, 9, v4\n"
    "\tv_bfe_u32 v3, v1, 3, 1\n"
    "\tv_lshl_add_u32 v180, v3, 7, v180\n"
    "\tv_bfe_u32 v3, v1, 2, 1\n"
    "\tv_lshl_add_u32 v180, v3, 11, v180\n"
    "\tv_bfe_u32 v3, v1, 0, 2\n"
    "\tv_lshl_add_u32 v180, v3, 3, v180\n"
    "\tv_bfe_u32 v3, v4, 0, 1\n"
    "\tv_lshl_add_u32 v180, v3, 5, v180\n"
    "\tv_bfe_u32 v3, v1, 2, 1\n"
    "\tv_lshl_add_u32 v180, v3, 6, v180\n"
    "\tv_add_u32 v180, 141312, v180\n"
    "\tv_xor_b32 v181, 32, v180\n"
    "\tv_xor_b32 v182, 64, v180\n"
    "\tv_xor_b32 v183, 96, v180\n"
    "\tv_lshrrev_b32 v3, 2, v2\n"
    "\tv_mov_b32 v7, 0x3f803f80\n"
    "\tv_cmp_eq_u32 vcc, 0, v3\n"
    "\ts_nop 1\n"
    "\tv_cndmask_b32 v184, 0, v7, vcc\n"
    "\tv_mov_b32 v185, v184\n"
    "\tv_mov_b32 v186, v184\n"
    "\tv_mov_b32 v187, v184\n"
    "\tv_cmp_eq_u32 vcc, 1, v3\n"
    "\ts_nop 1\n"
    "\tv_cndmask_b32 v188, 0, v7, vcc\n"
    "\tv_mov_b32 v189, v188\n"
    "\tv_mov_b32 v190, v188\n"
    "\tv_mov_b32 v191, v188\n"
    "\tv_cmp_eq_u32 vcc, 2, v3\n"
    "\ts_nop 1\n"
    "\tv_cndmask_b32 v192, 0, v7, vcc\n"
    "\tv_mov_b32 v193, v192\n"
    "\tv_mov_b32 v194, v192\n"
    "\tv_mov_b32 v195, v192\n"
    "\tv_cmp_eq_u32 vcc, 3, v3\n"
    "\ts_nop 1\n"
    "\tv_cndmask_b32 v196, 0, v7, vcc\n"
    "\tv_mov_b32 v197, v196\n"
    "\tv_mov_b32 v198, v196\n"
    "\tv_mov_b32 v199, v196\n"
    "\tv_mov_b64 v[160:161], 0\n"
    "\tv_mov_b64 v[162:163], 0\n"
    "\tv_mov_b64 v[164:165], 0\n"
    "\tv_mov_b64 v[166:167], 0\n"
    "\ts_mov_b32 s56, 0x05040100\n"
    "\ts_mov_b32 s57, 0x07060302\n"
    "\ts_waitcnt lgkmcnt(0)\n"
    "\ts_add_u32 s70, s70, 0x7f4a7c15\n"
    "\ts_addc_u32 s71, s71, 0x9e3779b9\n"
    "\ts_and_b32 s72, s71, -2\n"
    "\ts_or_b32 s58, s72, 1\n"
    "\tv_mov_b32 v3, 161052\n"
    "\tv_mov_b32 v7, s58\n"
    "\tds_write_b32 v3, v7\n"
    "\ts_waitcnt vmcnt(0)\n"
    "\tds_read_b64 v[152:153], v177\n"
    "\tds_read_b64 v[154:155], v177 offset:32\n"
    "\tds_read_b64_tr_b16 v[56:57], v180 offset:0\n"
    "\tds_read_b64_tr_b16 v[58:59], v180 offset:256\n"
    "\tds_read_b64_tr_b16 v[60:61], v180 offset:4096\n"
    "\tds_read_b64_tr_b16 v[62:63], v180 offset:4352\n"
    "\tds_read_b64_tr_b16 v[64:65], v180 offset:8192\n"
    "\tds_read_b64_tr_b16 v[66:67], v180 offset:8448\n"
    "\tds_read_b64_tr_b16 v[68:69], v180 offset:12288\n"
    "\tds_read_b64_tr_b16 v[70:71], v180 offset:12544\n"
    "\ts_waitcnt lgkmcnt(4)\n"
    "\tv_perm_b32 v156, v154, v152, s56\n"
    "\tv_perm_b32 v157, v154, v152, s57\n"
    "\tv_perm_b32 v158, v155, v153, s56\n"
    "\tv_perm_b32 v159, v155, v153, s57\n"
    "\tds_write2_b32 v178, v156, v157 offset1:4\n"
    "\tds_write2_b32 v178, v158, v159 offset0:1 offset1:5\n"
    "\tds_read_b64_tr_b16 v[72:73], v181 offset:0\n"
    "\tds_read_b64_tr_b16 v[74:75], v181 offset:256\n"
    "\tds_read_b64_tr_b16 v[76:77], v181 offset:4096\n"
    "\tds_read_b64_tr_b16 v[78:79], v181 offset:4352\n"
    "\tds_read_b64_tr_b16 v[80:81], v181 offset:8192\n"
    "\tds_read_b64_tr_b16 v[82:83], v181 offset:8448\n"
    "\tds_read_b64_tr_b16 v[84:85], v181 offset:12288\n"
    "\tds_read_b64_tr_b16 v[86:87], v181 offset:12544\n"
    "\ts_waitcnt lgkmcnt(4)\n"
    "\tds_read_b64_tr_b16 v[88:89], v182 offset:0\n"
    "\tds_read_b64_tr_b16 v[90:91], v182 offset:256\n"
    "\tds_read_b64_tr_b16 v[92:93], v182 offset:4096\n"
    "\tds_read_b64_tr_b16 v[94:95], v182 offset:4352\n"
    "\tds_read_b64_tr_b16 v[96:97], v182 offset:8192\n"
    "\tds_read_b64_tr_b16 v[98:99], v182 offset:8448\n"
    "\tds_read_b64_tr_b16 v[100:101], v182 offset:12288\n"
    "\tds_read_b64_tr_b16 v[102:103], v182 offset:12544\n"
    "\ts_waitcnt lgkmcnt(4)\n"
    "\tds_read_b64_tr_b16 v[104:105], v183 offset:0\n"
    "\tds_read_b64_tr_b16 v[106:107], v183 offset:256\n"
    "\tds_read_b64_tr_b16 v[108:109], v183 offset:4096\n"
    "\tds_read_b64_tr_b16 v[110:111], v183 offset:4352\n"
    "\tds_read_b64_tr_b16 v[112:113], v183 offset:8192\n"
    "\tds_read_b64_tr_b16 v[114:115], v183 offset:8448\n"
    "\tds_read_b64_tr_b16 v[116:117], v183 offset:12288\n"
    "\tds_read_b64_tr_b16 v[118:119], v183 offset:12544\n"
    "\ts_waitcnt lgkmcnt(4)\n"
    "\ts_waitcnt lgkmcnt(0)\n"
    "\tv_mov_b32 v3, 161064\n"
    "\tv_mov_b32 v7, 1\n"
    "\ts_mov_b64 exec, 1\n"
    "\tds_add_u32 v3, v7\n"
    "\ts_mov_b64 exec, -1\n"
    "\tv_smfmac_f32_16x16x64_bf16 v[164:167], v[184:187], v[56:63], v22\n"
    "\tv_smfmac_f32_16x16x64_bf16 v[164:167], v[184:187], v[64:71], v22\n"
    "\tv_smfmac_f32_16x16x64_bf16 v[164:167], v[188:191], v[72:79], v22\n"
    "\tv_smfmac_f32_16x16x64_bf16 v[164:167], v[188:191], v[80:87], v22\n"
    "\tv_smfmac_f32_16x16x64_bf16 v[164:167], v[192:195], v[88:95], v22\n"
    "\tv_smfmac_f32_16x16x64_bf16 v[164:167], v[192:195], v[96:103], v22\n"
    "\tv_smfmac_f32_16x16x64_bf16 v[164:167], v[196:199], v[104:111], v22\n"
    "\tv_smfmac_f32_16x16x64_bf16 v[164:167], v[196:199], v[112:119], v22\n"
    "\ts_nop 7\n"
    "\tv_add_f32 v23, v164, v165\n"
    "\tds_read_b128 v[152:155], v21\n"
    "\tds_read_b128 v[120:123], v40 offset:0\n"
    "\tds_read_b128 v[124:127], v40 offset:128\n"
    "\tds_read_b128 v[128:131], v41 offset:0\n"
    "\tds_read_b128 v[132:135], v41 offset:128\n"
    "\tds_read_b128 v[136:139], v42 offset:0\n"
    "\tds_read_b128 v[140:143], v42 offset:128\n"
    "\tds_read_b128 v[144:147], v43 offset:0\n"
    "\tds_read_b128 v[148:151], v43 offset:128\n"
    "\ts_mov_b32 s56, 0x3f803f80\n"
    "\tv_mov_b32 v158, 0x3b124925\n"
    "\ts_waitcnt lgkmcnt(8)\n"
    "\tv_dot2_f32_bf16 v156, v152, s56, 0\n"
    "\tv_dot2_f32_bf16 v157, v152, v152, 0\n"
    "\tv_dot2_f32_bf16 v156, v153, s56, v156\n"
    "\tv_dot2_f32_bf16 v157, v153, v153, v157\n"
    "\tv_dot2_f32_bf16 v156, v154, s56, v156\n"
    "\tv_dot2_f32_bf16 v157, v154, v154, v157\n"
    "\tv_dot2_f32_bf16 v156, v155, s56, v156\n"
    "\tv_dot2_f32_bf16 v157, v155, v155, v157\n"
    "\ts_waitcnt lgkmcnt(7)\n"
    "\tv_smfmac_f32_16x16x64_bf16 v[160:163], v[120:123], v[56:63], v22\n"
    "\tv_add_f32_dpp v156, v156, v156 quad_perm:[1,0,3,2] row_mask:0xf bank_mask:0xf\n"
    "\tv_add_f32_dpp v157, v157, v157 quad_perm:[1,0,3,2] row_mask:0xf bank_mask:0xf\n"
    "\ts_waitcnt lgkmcnt(6)\n"
    "\tv_smfmac_f32_16x16x64_bf16 v[160:163], v[124:127], v[64:71], v22\n"
    "\tv_add_f32_dpp v156, v156, v156 quad_perm:[2,3,0,1] row_mask:0xf bank_mask:0xf\n"
    "\tv_add_f32_dpp v157, v157, v157 quad_perm:[2,3,0,1] row_mask:0xf bank_mask:0xf\n"
    "\ts_waitcnt lgkmcnt(5)\n"
    "\tv_smfmac_f32_16x16x64_bf16 v[160:163], v[128:131], v[72:79], v22\n"
    "\tv_add_f32_dpp v156, v156, v156 row_half_mirror row_mask:0xf bank_mask:0xf\n"
    "\tv_add_f32_dpp v157, v157, v157 row_half_mirror row_mask:0xf bank_mask:0xf\n"
    "\ts_waitcnt lgkmcnt(4)\n"
    "\tv_smfmac_f32_16x16x64_bf16 v[160:163], v[132:135], v[80:87], v22\n"
    "\tv_add_f32_dpp v156, v156, v156 row_mirror row_mask:0xf bank_mask:0xf\n"
    "\tv_add_f32_dpp v157, v157, v157 row_mirror row_mask:0xf bank_mask:0xf\n"
    "\ts_waitcnt lgkmcnt(3)\n"
    "\tv_smfmac_f32_16x16x64_bf16 v[160:163], v[136:139], v[88:95], v22\n"
    "\tv_add_f32_dpp v156, v156, v156 row_bcast:15 row_mask:0xa bank_mask:0xf\n"
    "\tv_add_f32_dpp v157, v157, v157 row_bcast:15 row_mask:0xa bank_mask:0xf\n"
    "\ts_waitcnt lgkmcnt(2)\n"
    "\tv_smfmac_f32_16x16x64_bf16 v[160:163], v[140:143], v[96:103], v22\n"
    "\tv_add_f32_dpp v156, v156, v156 row_bcast:31 row_mask:0xc bank_mask:0xf\n"
    "\tv_add_f32_dpp v157, v157, v157 row_bcast:31 row_mask:0xc bank_mask:0xf\n"
    "\ts_waitcnt lgkmcnt(1)\n"
    "\tv_smfmac_f32_16x16x64_bf16 v[160:163], v[144:147], v[104:111], v22\n"
    "\tv_readlane_b32 s58, v156, 63\n"
    "\tv_readlane_b32 s59, v157, 63\n"
    "\ts_waitcnt lgkmcnt(0)\n"
    "\tv_smfmac_f32_16x16x64_bf16 v[160:163], v[148:151], v[112:119], v22\n"
    "\tv_mul_f32 v152, s58, v158\n"
    "\tv_mul_f32 v153, s59, v158\n"
    "\tv_fma_f32 v153, -v152, v152, v153\n"
    "\tv_max_f32 v153, 0, v153\n"
    "\tv_add_f32 v153, 0x3727c5ac, v153\n"
    "\tv_rsq_f32 v153, v153\n"
    "\ts_nop 1\n"
    "\tv_add_f32 v158, v160, v161\n"
    "\tv_fma_f32 v158, -v152, v23, v158\n"
    "\tv_mul_f32 v158, v158, v153\n"
    "\ts_nop 1\n"
    "\tv_mov_b32_dpp v159, v158 quad_perm:[1,0,3,2] row_mask:0xf bank_mask:0xf\n"
    "\tv_cvt_pk_bf16_f32 v158, v158, v159\n"
    "\tv_mov_b32 v159, s72\n"
    "\tbuffer_store_dwordx2 v[158:159], v176, s[36:39], 0 offen sc1\n"
    "L%=_s_mid:\n"
    "\tv_and_b32 v3, 1, v1\n"
    "\tv_lshlrev_b32 v3, 4, v3\n"
    "\tv_mov_b32 v7, 0xffff\n"
    "\tv_lshlrev_b32 v7, v3, v7\n"
    "\tv_lshrrev_b32 v3, 2, v2\n"
    "\tv_cmp_eq_u32 vcc, 0, v3\n"
    "\ts_nop 1\n"
    "\tv_cndmask_b32 v44, 0, v7, vcc\n"
    "\tv_and_b32 v48, 0x3f803f80, v44\n"
    "\tv_cmp_eq_u32 vcc, 1, v3\n"
    "\ts_nop 1\n"
    "\tv_cndmask_b32 v45, 0, v7, vcc\n"
    "\tv_and_b32 v49, 0x3f803f80, v45\n"
    "\tv_cmp_eq_u32 vcc, 2, v3\n"
    "\ts_nop 1\n"
    "\tv_cndmask_b32 v46, 0, v7, vcc\n"
    "\tv_and_b32 v50, 0x3f803f80, v46\n"
    "\tv_cmp_eq_u32 vcc, 3, v3\n"
    "\ts_nop 1\n"
    "\tv_cndmask_b32 v47, 0, v7, vcc\n"
    "\tv_and_b32 v51, 0x3f803f80, v47\n"
    "\ts_lshl_b32 s58, s48, 8\n"
    "\ts_add_u32 s57, s58, 157696\n"
    "\tv_lshrrev_b32 v3, 1, v2\n"
    "\tv_lshlrev_b32 v24, 5, v3\n"
    "\tv_bfe_u32 v3, v4, 0, 1\n"
    "\tv_lshl_add_u32 v24, v3, 4, v24\n"
    "\tv_bfe_u32 v3, v2, 0, 1\n"
    "\tv_lshl_add_u32 v24, v3, 3, v24\n"
    "\tv_lshl_add_u32 v24, v5, 2, v24\n"
    "\tv_add_u32 v24, s57, v24\n"
    "\ts_lshl_b32 s56, s48, 8\n"
    "\ts_cmp_eq_u32 s48, 3\n"
    "\ts_cselect_b32 s57, 8, 16\n"
    "\tv_lshlrev_b32 v26, 10, v4\n"
    "\tv_lshl_add_u32 v26, v2, 4, v26\n"
    "\tv_add_u32 v26, s56, v26\n"
    "\tv_mov_b32 v7, 1073741824\n"
    "\tv_cmp_gt_u32 vcc, s57, v2\n"
    "\ts_nop 1\n"
    "\tv_cndmask_b32 v26, v7, v26, vcc\n"
    "\tv_add_u32 v27, 4096, v26\n"
    "\tv_mov_b32 v7, 1073741824\n"
    "\tv_cmp_gt_u32 vcc, 16, v1\n"
    "\ts_nop 1\n"
    "\tv_cndmask_b32 v28, v7, v27, vcc\n"
    "\ts_mul_i32 s57, s27, 11\n"
    "\ts_lshr_b32 s57, s57, 5\n"
    "\ts_mul_i32 s57, s57, 3\n"
    "\ts_sub_u32 s57, s27, s57\n"
    "\ts_lshl_b32 s57, s57, 3\n"
    "\ts_add_u32 s57, s57, s26\n"
    "\ts_lshl_b32 s57, s57, 6\n"
    "\ts_add_u32 s57, s57, 1409024\n"
    "\tv_mov_b32 v38, s57\n"
    "\ts_cmp_ge_u32 s27, 28\n"
    "\ts_cbranch_scc1 L%=_b_svc\n"
    "\ts_add_u32 s57, s58, 154624\n"
    "\tv_lshrrev_b32 v3, 1, v2\n"
    "\tv_lshlrev_b32 v31, 5, v3\n"
    "\tv_bfe_u32 v3, v4, 0, 1\n"
    "\tv_lshl_add_u32 v31, v3, 4, v31\n"
    "\tv_bfe_u32 v3, v2, 0, 1\n"
    "\tv_lshl_add_u32 v31, v3, 3, v31\n"
    "\tv_lshl_add_u32 v31, v5, 2, v31\n"
    "\tv_add_u32 v31, s57, v31\n"
    "\ts_lshl_b32 s57, s26, 11\n"
    "\ts_lshr_b32 s61, s57, 13\n"
    "\ts_lshl_b32 s61, s61, 13\n"
    "\ts_add_u32 s57, s57, s61\n"
    "\ts_add_u32 s57, s57, 524288\n"
    "\ts_lshl_b32 s59, s76, 3\n"
    "\ts_add_u32 s57, s57, s59\n"
    "\tv_lshlrev_b32 v29, 8, v2\n"
    "\tv_lshl_add_u32 v29, v4, 6, v29\n"
    "\tv_add_u32 v29, s57, v29\n"
    "\tv_mov_b32 v7, 1073741824\n"
    "\tv_cmp_gt_u32 vcc, 7, v2\n"
    "\ts_nop 1\n"
    "\tv_cndmask_b32 v29, v7, v29, vcc\n"
    "\tv_cmp_gt_u32 s[66:67], 7, v2\n"
    "\ts_lshl_b32 s59, s26, 1\n"
    "\ts_add_u32 s59, s59, s76\n"
    "\ts_lshl_b32 s59, s59, 15\n"
    "\ts_add_u32 s59, s59, s58\n"
    "\ts_sub_u32 s59, s59, 256\n"
    "\tv_lshlrev_b32 v30, 9, v4\n"
    "\tv_lshl_add_u32 v30, v2, 4, v30\n"
    "\tv_add_u32 v30, s59, v30\n"
    "\ts_cmp_eq_u32 s48, 3\n"
    "\ts_cbranch_scc1 L%=_as_w7\n"
    "\ts_cmp_lg_u32 s48, 1\n"
    "\ts_cbranch_scc1 L%=_as_na\n"
    "\ts_add_u32 s60, s74, 11500\n"
    "L%=_wc15:\n"
    "\ts_sleep 2\n"
    "\ts_memtime s[64:65]\n"
    "\ts_waitcnt lgkmcnt(0)\n"
    "\ts_sub_u32 s61, s64, s60\n"
    "\ts_cmp_lt_i32 s61, 0\n"
    "\ts_cbranch_scc1 L%=_wc15\n"
    "\tv_lshlrev_b32 v153, 7, v6\n"
    "\tv_add_u32 v153, 1556480, v153\n"
    "\ts_mov_b32 s73, 0\n"
    "\tbuffer_load_dwordx4 v[176:179], v153, s[36:39], 0 offen sc1\n"
    "L%=_as_ar:\n"
    "\ts_add_u32 s73, s73, 1\n"
    "\ts_waitcnt vmcnt(0)\n"
    "\tv_xor_b32 v154, s70, v176\n"
    "\tv_xor_b32 v155, s71, v177\n"
    "\tv_xor_b32 v156, s71, v179\n"
    "\tv_or3_b32 v154, v154, v155, v156\n"
    "\tv_cmp_ne_u32 vcc, 0, v154\n"
    "\ts_cbranch_vccz L%=_as_arok\n"
    "\ts_cmp_ge_u32 s73, 40000\n"
    "\ts_cbranch_scc1 L%=_as_arok\n"
    "\ts_sleep 4\n"
    "\tbuffer_load_dwordx4 v[176:179], v153, s[36:39], 0 offen sc1\n"
    "\ts_branch L%=_as_ar\n"
    "L%=_as_arok:\n"
    "\tv_xor_b32 v184, s70, v178\n"
    "\ts_nop 1\n"
    "\tv_add_u32_dpp v184, v184, v184 quad_perm:[1,0,3,2] row_mask:0xf bank_mask:0xf\n"
    "\ts_nop 1\n"
    "\tv_add_u32_dpp v184, v184, v184 quad_perm:[2,3,0,1] row_mask:0xf bank_mask:0xf\n"
    "\ts_nop 1\n"
    "\tv_add_u32_dpp v184, v184, v184 row_half_mirror row_mask:0xf bank_mask:0xf\n"
    "\ts_nop 1\n"
    "\tv_add_u32_dpp v184, v184, v184 row_mirror row_mask:0xf bank_mask:0xf\n"
    "\ts_nop 1\n"
    "\tv_add_u32_dpp v184, v184, v184 row_bcast:15 row_mask:0xa bank_mask:0xf\n"
    "\ts_nop 1\n"
    "\tv_add_u32_dpp v184, v184, v184 row_bcast:31 row_mask:0xc bank_mask:0xf\n"
    "\ts_nop 0\n"
    "\tv_readlane_b32 s60, v184, 63\n"
    "\ts_nop 0\n"
    "\ts_cmp_gt_i32 s60, 0\n"
    "\ts_cselect_b32 s60, 1, 0\n"
    "\ts_xor_b32 s60, s60, s78\n"
    "\ts_lshl_b32 s60, s60, 13\n"
    "\tv_mov_b32 v155, s60\n"
    "\tv_mov_b32 v3, 161024\n"
    "\tds_write_b32 v3, v155\n"
    "L%=_as_na:\n"
    "\ts_add_u32 s56, s27, 1\n"
    "\ts_cmp_eq_u32 s56, 28\n"
    "\ts_cselect_b32 s56, 0, s56\n"
    "\tv_mov_b32 v152, s56\n"
    "\tv_cmp_gt_u32 vcc, 28, v1\n"
    "\ts_nop 1\n"
    "\tv_cndmask_b32 v153, v152, v1, vcc\n"
    "\tv_cmp_eq_u32 vcc, s27, v1\n"
    "\ts_nop 1\n"
    "\tv_cndmask_b32 v153, v153, v152, vcc\n"
    "\ts_lshl_b32 s56, s26, 6\n"
    "\ts_add_u32 s56, s56, 1376256\n"
    "\tv_bfe_u32 v3, v153, 4, 1\n"
    "\tv_lshlrev_b32 v154, 14, v3\n"
    "\tv_bfe_u32 v3, v153, 0, 4\n"
    "\tv_lshl_add_u32 v154, v3, 9, v154\n"
    "\tv_add_u32 v154, s56, v154\n"
    "\ts_waitcnt lgkmcnt(0)\n"
    "\ts_barrier\n"
    "\tv_mov_b32 v3, 161024\n"
    "\tds_read_b32 v3, v3\n"
    "\ts_waitcnt lgkmcnt(0)\n"
    "\tv_readfirstlane_b32 s51, v3\n"
    "\ts_nop 4\n"
    "\tbuffer_load_dword v152, v154, s[36:39], s51 offen sc1\n"
    "\ts_mov_b32 s73, 0\n"
    "\ts_barrier\n"
    "\ts_memtime s[64:65]\n"
    "\ts_waitcnt lgkmcnt(0)\n"
    "\ts_add_u32 s59, s64, 700\n"
    "L%=_as_gw:\n"
    "\ts_waitcnt vmcnt(0)\n"
    "\tv_cmp_eq_u32 vcc, -1, v152\n"
    "\ts_cbranch_vccz L%=_as_gok\n"
    "\ts_add_u32 s73, s73, 1\n"
    "\ts_cmp_ge_u32 s73, 40000\n"
    "\ts_cbranch_scc1 L%=_as_gok\n"
    "\tbuffer_load_dword v152, v154, s[36:39], s51 offen sc1\n"
    "\ts_branch L%=_as_gw\n"
    "L%=_as_gok:\n"
    "\ts_memrealtime s[62:63]\n"
    "\ts_memtime s[64:65]\n"
    "\tv_readfirstlane_b32 s58, v152\n"
    "\ts_nop 1\n"
    "\tv_subrev_u32 v152, s58, v152\n"
    "\tv_bfe_i32 v152, v152, 0, 31\n"
    "\ts_nop 1\n"
    "\tv_max_i32_dpp v152, v152, v152 quad_perm:[1,0,3,2] row_mask:0xf bank_mask:0xf\n"
    "\ts_nop 1\n"
    "\tv_max_i32_dpp v152, v152, v152 quad_perm:[2,3,0,1] row_mask:0xf bank_mask:0xf\n"
    "\ts_nop 1\n"
    "\tv_max_i32_dpp v152, v152, v152 row_half_mirror row_mask:0xf bank_mask:0xf\n"
    "\ts_nop 1\n"
    "\tv_max_i32_dpp v152, v152, v152 row_mirror row_mask:0xf bank_mask:0xf\n"
    "\ts_nop 1\n"
    "\tv_max_i32_dpp v152, v152, v152 row_bcast:15 row_mask:0xa bank_mask:0xf\n"
    "\ts_nop 0\n"
    "\tv_readlane_b32 s60, v152, 31\n"
    "\ts_nop 0\n"
    "\ts_add_u32 s60, s60, s58\n"
    "\ts_add_u32 s60, s60, 65\n"
    "\ts_waitcnt lgkmcnt(0)\n"
    "\ts_sub_u32 s60, s60, s62\n"
    "\ts_bfe_i32 s60, s60, 2031616\n"
    "\ts_min_i32 s60, s60, 2000\n"
    "\ts_max_i32 s60, s60, 0\n"
    "\ts_mul_i32 s60, s60, 24\n"
    "\ts_add_u32 s60, s60, s64\n"
    "\ts_sub_u32 s61, s60, s59\n"
    "\ts_cmp_gt_i32 s61, 0\n"
    "\ts_cselect_b32 s60, s60, s59\n"
    "L%=_wc16:\n"
    "\ts_memtime s[64:65]\n"
    "\ts_waitcnt lgkmcnt(0)\n"
    "\ts_sub_u32 s61, s64, s60\n"
    "\ts_cmp_lt_i32 s61, 0\n"
    "\ts_cbranch_scc1 L%=_wc16\n"
    "L%=_as_hb:\n"
    "\ts_add_u32 s56, s50, s51\n"
    "\ts_add_u32 s57, s56, 4096\n"
    "\ts_add_u32 s58, s56, 16384\n"
    "\ts_add_u32 s59, s56, 20480\n"
    "\ts_mov_b32 s73, 0\n"
    "\tbuffer_load_dwordx2 v[176:177], v29, s[36:39], s56 offen sc1\n"
    "\tbuffer_load_dwordx4 v[180:183], v30, s[36:39], s56 offen offset:0 sc1\n"
    "\tbuffer_load_dwordx4 v[184:187], v30, s[36:39], s56 offen offset:2048 sc1\n"
    "\tbuffer_load_dwordx4 v[188:191], v30, s[36:39], s57 offen offset:0 sc1\n"
    "\tbuffer_load_dwordx4 v[192:195], v30, s[36:39], s57 offen offset:2048 sc1\n"
    "\tbuffer_load_dwordx4 v[196:199], v30, s[36:39], s58 offen offset:0 sc1\n"
    "\tbuffer_load_dwordx4 v[200:203], v30, s[36:39], s58 offen offset:2048 sc1\n"
    "\tbuffer_load_dwordx4 v[204:207], v30, s[36:39], s59 offen offset:0 sc1\n"
    "\tv_mov_b32 v152, 0xff800000\n"
    "\ts_waitcnt vmcnt(7)\n"
    "\tv_cndmask_b32 v153, v152, v176, s[66:67]\n"
    "\tv_cndmask_b32 v152, v152, v176, s[66:67]\n"
    "\ts_nop 1\n"
    "\tv_max_f32_dpp v152, v152, v152 quad_perm:[1,0,3,2] row_mask:0xf bank_mask:0xf\n"
    "\ts_nop 1\n"
    "\tv_max_f32_dpp v152, v152, v152 quad_perm:[2,3,0,1] row_mask:0xf bank_mask:0xf\n"
    "\ts_nop 1\n"
    "\tv_max_f32_dpp v152, v152, v152 row_half_mirror row_mask:0xf bank_mask:0xf\n"
    "\ts_nop 1\n"
    "\tv_max_f32_dpp v152, v152, v152 row_mirror row_mask:0xf bank_mask:0xf\n"
    "\ts_nop 1\n"
    "\tv_max_f32_dpp v152, v152, v152 row_bcast:15 row_mask:0xa bank_mask:0xf\n"
    "\ts_nop 1\n"
    "\tv_max_f32_dpp v152, v152, v152 row_bcast:31 row_mask:0xc bank_mask:0xf\n"
    "\ts_nop 0\n"
    "\tv_readlane_b32 s60, v152, 63\n"
    "\ts_nop 1\n"
    "\tv_subrev_f32 v153, s60, v153\n"
    "\tv_exp_f32 v153, v153\n"
    "\ts_nop 0\n"
    "\tv_cvt_pk_bf16_f32 v154, v153, v153\n"
    "\tv_mul_f32 v155, v153, v177\n"
    "\ts_nop 0\n"
    "\tv_and_b32_dpp v208, v154, v44 row_newbcast:0 row_mask:0xf bank_mask:0xf\n"
    "\tv_and_b32_dpp v209, v154, v45 row_newbcast:0 row_mask:0xf bank_mask:0xf\n"
    "\tv_and_b32_dpp v210, v154, v46 row_newbcast:0 row_mask:0xf bank_mask:0xf\n"
    "\tv_and_b32_dpp v211, v154, v47 row_newbcast:0 row_mask:0xf bank_mask:0xf\n"
    "\tv_add_f32_dpp v155, v155, v155 quad_perm:[1,0,3,2] row_mask:0xf bank_mask:0xf\n"
    "\ts_waitcnt vmcnt(6)\n"
    "\tv_mfma_f32_16x16x32_bf16 v[160:163], v[208:211], v[180:183], 0\n"
    "\tv_and_b32_dpp v212, v154, v44 row_newbcast:1 row_mask:0xf bank_mask:0xf\n"
    "\tv_and_b32_dpp v213, v154, v45 row_newbcast:1 row_mask:0xf bank_mask:0xf\n"
    "\tv_and_b32_dpp v214, v154, v46 row_newbcast:1 row_mask:0xf bank_mask:0xf\n"
    "\tv_and_b32_dpp v215, v154, v47 row_newbcast:1 row_mask:0xf bank_mask:0xf\n"
    "\tv_add_f32_dpp v155, v155, v155 quad_perm:[2,3,0,1] row_mask:0xf bank_mask:0xf\n"
    "\ts_waitcnt vmcnt(5)\n"
    "\tv_mfma_f32_16x16x32_bf16 v[160:163], v[212:215], v[184:187], v[160:163]\n"
    "\tv_and_b32_dpp v208, v154, v44 row_newbcast:2 row_mask:0xf bank_mask:0xf\n"
    "\tv_and_b32_dpp v209, v154, v45 row_newbcast:2 row_mask:0xf bank_mask:0xf\n"
    "\tv_and_b32_dpp v210, v154, v46 row_newbcast:2 row_mask:0xf bank_mask:0xf\n"
    "\tv_and_b32_dpp v211, v154, v47 row_newbcast:2 row_mask:0xf bank_mask:0xf\n"
    "\tv_add_f32_dpp v155, v155, v155 row_half_mirror row_mask:0xf bank_mask:0xf\n"
    "\ts_waitcnt vmcnt(4)\n"
    "\tv_mfma_f32_16x16x32_bf16 v[160:163], v[208:211], v[188:191], v[160:163]\n"
    "\tv_and_b32_dpp v212, v154, v44 row_newbcast:3 row_mask:0xf bank_mask:0xf\n"
    "\tv_and_b32_dpp v213, v154, v45 row_newbcast:3 row_mask:0xf bank_mask:0xf\n"
    "\tv_and_b32_dpp v214, v154, v46 row_newbcast:3 row_mask:0xf bank_mask:0xf\n"
    "\tv_and_b32_dpp v215, v154, v47 row_newbcast:3 row_mask:0xf bank_mask:0xf\n"
    "\tv_add_f32_dpp v155, v155, v155 row_mirror row_mask:0xf bank_mask:0xf\n"
    "\ts_waitcnt vmcnt(3)\n"
    "\tv_mfma_f32_16x16x32_bf16 v[160:163], v[212:215], v[192:195], v[160:163]\n"
    "\tv_and_b32_dpp v208, v154, v44 row_newbcast:4 row_mask:0xf bank_mask:0xf\n"
    "\tv_and_b32_dpp v209, v154, v45 row_newbcast:4 row_mask:0xf bank_mask:0xf\n"
    "\tv_and_b32_dpp v210, v154, v46 row_newbcast:4 row_mask:0xf bank_mask:0xf\n"
    "\tv_and_b32_dpp v211, v154, v47 row_newbcast:4 row_mask:0xf bank_mask:0xf\n"
    "\tv_add_f32_dpp v155, v155, v155 row_bcast:15 row_mask:0xa bank_mask:0xf\n"
    "\ts_waitcnt vmcnt(2)\n"
    "\tv_mfma_f32_16x16x32_bf16 v[160:163], v[208:211], v[196:199], v[160:163]\n"
    "\tv_and_b32_dpp v212, v154, v44 row_newbcast:5 row_mask:0xf bank_mask:0xf\n"
    "\tv_and_b32_dpp v213, v154, v45 row_newbcast:5 row_mask:0xf bank_mask:0xf\n"
    "\tv_and_b32_dpp v214, v154, v46 row_newbcast:5 row_mask:0xf bank_mask:0xf\n"
    "\tv_and_b32_dpp v215, v154, v47 row_newbcast:5 row_mask:0xf bank_mask:0xf\n"
    "\tv_add_f32_dpp v155, v155, v155 row_bcast:31 row_mask:0xc bank_mask:0xf\n"
    "\ts_waitcnt vmcnt(1)\n"
    "\tv_mfma_f32_16x16x32_bf16 v[160:163], v[212:215], v[200:203], v[160:163]\n"
    "\tv_and_b32_dpp v208, v154, v44 row_newbcast:6 row_mask:0xf bank_mask:0xf\n"
    "\tv_and_b32_dpp v209, v154, v45 row_newbcast:6 row_mask:0xf bank_mask:0xf\n"
    "\tv_and_b32_dpp v210, v154, v46 row_newbcast:6 row_mask:0xf bank_mask:0xf\n"
    "\tv_and_b32_dpp v211, v154, v47 row_newbcast:6 row_mask:0xf bank_mask:0xf\n"
    "\tv_readlane_b32 s60, v155, 63\n"
    "\ts_waitcnt vmcnt(0)\n"
    "\tv_mfma_f32_16x16x32_bf16 v[160:163], v[208:211], v[204:207], v[160:163]\n"
    "\tv_rcp_f32 v155, s60\n"
    "\ts_nop 6\n"
    "\tv_cmp_u_f32 vcc, v160, v155\n"
    "\ts_cbranch_vccnz L%=_ps19\n"
    "L%=_pd18:\n"
    "\ts_nop 3\n"
    "\tv_mul_f32 v160, v160, v155\n"
    "\tv_mul_f32 v161, v161, v155\n"
    "\tv_cvt_pk_bf16_f32 v160, v160, v161\n"
    "\tds_write_b32 v31, v160\n"
    "\ts_waitcnt lgkmcnt(0)\n"
    "\ts_barrier\n"
    "\ts_cmp_eq_u32 s52, 0\n"
    "\ts_cbranch_scc1 L%=_as_l0\n"
    "\ts_cmp_lg_u32 s48, 1\n"
    "\ts_cbranch_scc1 L%=_exit\n"
    "\ts_cmp_ge_u32 s53, 4\n"
    "\ts_cbranch_scc1 L%=_exit\n"
    "\ts_lshl_b32 s56, s53, 8\n"
    "\ts_cmp_eq_u32 s53, 3\n"
    "\ts_cselect_b32 s57, 8, 16\n"
    "\tv_lshlrev_b32 v26, 10, v4\n"
    "\tv_lshl_add_u32 v26, v2, 4, v26\n"
    "\tv_add_u32 v26, s56, v26\n"
    "\tv_mov_b32 v7, 1073741824\n"
    "\tv_cmp_gt_u32 vcc, s57, v2\n"
    "\ts_nop 1\n"
    "\tv_cndmask_b32 v26, v7, v26, vcc\n"
    "\tv_add_u32 v27, 4096, v26\n"
    "\tv_mov_b32 v7, 1073741824\n"
    "\tv_cmp_gt_u32 vcc, 16, v1\n"
    "\ts_nop 1\n"
    "\tv_cndmask_b32 v28, v7, v27, vcc\n"
    "\ts_mov_b32 s40, s22\n"
    "\ts_and_b32 s41, s23, 0xffff\n"
    "\ts_mov_b32 s42, 896\n"
    "\ts_mov_b32 s43, 0x20000\n"
    "\tv_lshrrev_b32 v3, 2, v2\n"
    "\tv_lshlrev_b32 v24, 6, v3\n"
    "\tv_bfe_u32 v3, v2, 0, 2\n"
    "\tv_lshl_add_u32 v24, v3, 3, v24\n"
    "\tv_lshl_add_u32 v24, v4, 1, v24\n"
    "\tv_add_u32 v24, s56, v24\n"
    "\ts_add_u32 s56, s50, s51\n"
    "\ts_add_u32 s59, s56, 638976\n"
    "\ts_add_u32 s58, s56, 622592\n"
    "\ts_add_u32 s57, s56, 606208\n"
    "\ts_add_u32 s56, s56, 589824\n"
    "\ts_mov_b32 s73, 0\n"
    "\ts_sleep 53\n"
    "\ts_nop 1\n"
    "\tbuffer_load_dwordx4 v[176:179], v26, s[36:39], s56 offen sc1\n"
    "\tbuffer_load_dwordx4 v[180:183], v27, s[36:39], s56 offen sc1\n"
    "\tbuffer_load_dwordx4 v[184:187], v26, s[36:39], s57 offen sc1\n"
    "\tbuffer_load_dwordx4 v[188:191], v27, s[36:39], s57 offen sc1\n"
    "\tbuffer_load_dwordx4 v[192:195], v26, s[36:39], s58 offen sc1\n"
    "\tbuffer_load_dwordx4 v[196:199], v27, s[36:39], s58 offen sc1\n"
    "\tbuffer_load_dwordx4 v[200:203], v26, s[36:39], s59 offen sc1\n"
    "\tbuffer_load_dwordx4 v[204:207], v28, s[36:39], s59 offen sc1\n"
    "\ts_waitcnt vmcnt(7)\n"
    "\tv_mfma_f32_16x16x32_bf16 v[160:163], v[48:51], v[176:179], 0\n"
    "\ts_waitcnt vmcnt(6)\n"
    "\tv_mfma_f32_16x16x32_bf16 v[160:163], v[48:51], v[180:183], v[160:163]\n"
    "\ts_waitcnt vmcnt(5)\n"
    "\tv_mfma_f32_16x16x32_bf16 v[160:163], v[48:51], v[184:187], v[160:163]\n"
    "\ts_waitcnt vmcnt(4)\n"
    "\tv_mfma_f32_16x16x32_bf16 v[160:163], v[48:51], v[188:191], v[160:163]\n"
    "\ts_waitcnt vmcnt(3)\n"
    "\tv_mfma_f32_16x16x32_bf16 v[160:163], v[48:51], v[192:195], v[160:163]\n"
    "\ts_waitcnt vmcnt(2)\n"
    "\tv_mfma_f32_16x16x32_bf16 v[160:163], v[48:51], v[196:199], v[160:163]\n"
    "\ts_waitcnt vmcnt(1)\n"
    "\tv_mfma_f32_16x16x32_bf16 v[160:163], v[48:51], v[200:203], v[160:163]\n"
    "\ts_waitcnt vmcnt(0)\n"
    "\tv_mfma_f32_16x16x32_bf16 v[160:163], v[48:51], v[204:207], v[160:163]\n"
    "\ts_nop 7\n"
    "\tv_cmp_u_f32 vcc, v160, v160\n"
    "\ts_cbranch_vccnz L%=_ps22\n"
    "L%=_pd21:\n"
    "\ts_nop 4\n"
    "\tv_cvt_pk_bf16_f32 v160, v160, v161\n"
    "\tbuffer_store_short v160, v24, s[40:43], 0 offen\n"
    "\tbuffer_store_short_d16_hi v160, v24, s[40:43], 0 offen offset:32\n"
    "\ts_cmp_eq_u32 s53, 0\n"
    "\ts_cselect_b32 s56, 1560576, 1073741824\n"
    "\tv_mov_b32 v156, s70\n"
    "\tv_mov_b32 v157, s71\n"
    "\tv_mov_b32 v3, s56\n"
    "\tbuffer_store_dwordx2 v[156:157], v3, s[36:39], 0 offen sc1\n"
    "\ts_endpgm\n"
    "L%=_as_l0:\n"
    "\ts_cmp_lg_u32 s48, 1\n"
    "\ts_cbranch_scc1 L%=_as_bt\n"
    "L%=_as_w1:\n"
    "\ts_mov_b32 s73, 0\n"
    "L%=_as_hc:\n"
    "\tbuffer_load_dword v152, v38, s[36:39], s51 offen sc1\n"
    "\ts_add_u32 s73, s73, 1\n"
    "\ts_waitcnt vmcnt(0)\n"
    "\tv_readfirstlane_b32 s75, v152\n"
    "\ts_nop 0\n"
    "\ts_cmp_lg_u32 s75, -1\n"
    "\ts_cselect_b32 s61, 40000, s73\n"
    "\ts_cmp_lt_u32 s61, 40000\n"
    "\ts_cbranch_scc1 L%=_as_hc\n"
    "\ts_memrealtime s[62:63]\n"
    "\ts_memtime s[64:65]\n"
    "\ts_add_u32 s60, s75, 240\n"
    "\ts_waitcnt lgkmcnt(0)\n"
    "\ts_sub_u32 s60, s60, s62\n"
    "\ts_bfe_i32 s60, s60, 2031616\n"
    "\ts_min_i32 s60, s60, 2000\n"
    "\ts_max_i32 s60, s60, 0\n"
    "\ts_mul_i32 s60, s60, 24\n"
    "\ts_add_u32 s60, s60, s64\n"
    "L%=_wc23:\n"
    "\ts_memtime s[64:65]\n"
    "\ts_waitcnt lgkmcnt(0)\n"
    "\ts_sub_u32 s61, s64, s60\n"
    "\ts_cmp_lt_i32 s61, 0\n"
    "\ts_cbranch_scc1 L%=_wc23\n"
    "\ts_branch L%=_as_bt\n"
    "L%=_as_w7:\n"
    "\ts_lshl_b32 s56, s53, 6\n"
    "\ts_lshr_b32 s61, s56, 13\n"
    "\ts_lshl_b32 s61, s61, 13\n"
    "\ts_add_u32 s56, s56, s61\n"
    "\ts_add_u32 s56, s56, 1376256\n"
    "\tv_mov_b32 v152, s56\n"
    "\tv_mov_b32 v7, 1073741824\n"
    "\tv_cmp_eq_u32 vcc, 0, v1\n"
    "\ts_nop 1\n"
    "\tv_cndmask_b32 v152, v7, v152, vcc\n"
    "\ts_barrier\n"
    "\ts_memrealtime s[62:63]\n"
    "\ts_waitcnt lgkmcnt(0)\n"
    "\ts_and_b32 s56, s62, 0x7fffffff\n"
    "\tv_mov_b32 v153, s56\n"
    "\tbuffer_store_dword v153, v152, s[36:39], s50 offen sc1\n"
    "\tbuffer_store_dword v153, v152, s[36:39], s79 offen sc1\n"
    "\tv_mov_b32 v3, 161024\n"
    "\tds_read_b32 v3, v3\n"
    "\ts_waitcnt lgkmcnt(0)\n"
    "\tv_readfirstlane_b32 s51, v3\n"
    "\ts_barrier\n"
    "\ts_lshl_b32 s56, s26, 6\n"
    "\ts_add_u32 s56, s56, s27\n"
    "\ts_lshl_b32 s56, s56, 9\n"
    "\ts_lshr_b32 s61, s56, 13\n"
    "\ts_lshl_b32 s61, s61, 13\n"
    "\ts_add_u32 s56, s56, s61\n"
    "\ts_add_u32 s56, s56, 655360\n"
    "\tv_lshlrev_b32 v152, 15, v5\n"
    "\tv_lshl_add_u32 v152, v6, 4, v152\n"
    "\tv_add_u32 v152, s56, v152\n"
    "\tv_mov_b32 v154, -1\n"
    "\tv_mov_b32 v155, -1\n"
    "\tv_mov_b32 v156, -1\n"
    "\tv_mov_b32 v157, -1\n"
    "\ts_barrier\n"
    "\ts_sleep 9\n"
    "\ts_nop 5\n"
    "\tbuffer_store_dwordx4 v[154:157], v152, s[36:39], s50 offen sc1\n"
    "\tbuffer_store_dwordx4 v[154:157], v152, s[36:39], s79 offen sc1\n"
    "\ts_branch L%=_as_bt\n"
    "L%=_as_w4:\n"
    "\ts_waitcnt vmcnt(10)\n"
    "\ts_barrier\n"
    "\tv_mov_b32 v3, 161024\n"
    "\tds_read_b32 v3, v3\n"
    "\ts_waitcnt lgkmcnt(0)\n"
    "\tv_readfirstlane_b32 s51, v3\n"
    "\ts_waitcnt vmcnt(0)\n"
    "\ts_barrier\n"
    "L%=_as_wo:\n"
    "\tv_mov_b64 v[160:161], 0\n"
    "\tv_mov_b64 v[162:163], 0\n"
    "\tv_mov_b64 v[164:165], 0\n"
    "\tv_mov_b64 v[166:167], 0\n"
    "\ts_waitcnt lgkmcnt(0)\n"
    "\ts_barrier\n"
    "\tds_read_b128 v[120:123], v32 offset:0\n"
    "\tds_read_b128 v[124:127], v32 offset:128\n"
    "\tds_read_b128 v[128:131], v32 offset:256\n"
    "\tds_read_b128 v[132:135], v32 offset:384\n"
    "\ts_waitcnt lgkmcnt(3)\n"
    "\tv_smfmac_f32_16x16x64_bf16 v[160:163], v[120:123], v[56:63], v22\n"
    "\tv_smfmac_f32_16x16x64_bf16 v[164:167], v[120:123], v[88:95], v22\n"
    "\ts_waitcnt lgkmcnt(2)\n"
    "\tv_smfmac_f32_16x16x64_bf16 v[160:163], v[124:127], v[64:71], v22\n"
    "\tv_smfmac_f32_16x16x64_bf16 v[164:167], v[124:127], v[96:103], v22\n"
    "\ts_waitcnt lgkmcnt(1)\n"
    "\tv_smfmac_f32_16x16x64_bf16 v[160:163], v[128:131], v[72:79], v22\n"
    "\tv_smfmac_f32_16x16x64_bf16 v[164:167], v[128:131], v[104:111], v22\n"
    "\ts_waitcnt lgkmcnt(0)\n"
    "\tv_smfmac_f32_16x16x64_bf16 v[160:163], v[132:135], v[80:87], v22\n"
    "\tv_smfmac_f32_16x16x64_bf16 v[164:167], v[132:135], v[112:119], v22\n"
    "\ts_nop 6\n"
    "\tv_add_f32 v152, v160, v161\n"
    "\tv_add_f32 v153, v164, v165\n"
    "\tv_cvt_pk_bf16_f32 v152, v152, v153\n"
    "\tbuffer_store_dword v152, v33, s[36:39], s50 offen sc1\n"
    "\tbuffer_store_dword v152, v33, s[36:39], s79 offen sc1\n"
    "\ts_cmp_lg_u32 s52, 0\n"
    "\ts_cbranch_scc1 L%=_exit\n"
    "L%=_as_bt:\n"
    "\ts_add_u32 s56, s50, s51\n"
    "\ts_add_u32 s59, s56, 638976\n"
    "\ts_add_u32 s58, s56, 622592\n"
    "\ts_add_u32 s57, s56, 606208\n"
    "\ts_add_u32 s56, s56, 589824\n"
    "\ts_mov_b32 s73, 0\n"
    "\ts_barrier\n"
    "\tbuffer_load_dwordx4 v[176:179], v26, s[36:39], s56 offen sc1\n"
    "\tbuffer_load_dwordx4 v[180:183], v27, s[36:39], s56 offen sc1\n"
    "\tbuffer_load_dwordx4 v[184:187], v26, s[36:39], s57 offen sc1\n"
    "\tbuffer_load_dwordx4 v[188:191], v27, s[36:39], s57 offen sc1\n"
    "\tbuffer_load_dwordx4 v[192:195], v26, s[36:39], s58 offen sc1\n"
    "\tbuffer_load_dwordx4 v[196:199], v27, s[36:39], s58 offen sc1\n"
    "\tbuffer_load_dwordx4 v[200:203], v26, s[36:39], s59 offen sc1\n"
    "\tbuffer_load_dwordx4 v[204:207], v28, s[36:39], s59 offen sc1\n"
    "\ts_waitcnt vmcnt(7)\n"
    "\tv_mfma_f32_16x16x32_bf16 v[160:163], v[48:51], v[176:179], 0\n"
    "\ts_waitcnt vmcnt(6)\n"
    "\tv_mfma_f32_16x16x32_bf16 v[160:163], v[48:51], v[180:183], v[160:163]\n"
    "\ts_waitcnt vmcnt(5)\n"
    "\tv_mfma_f32_16x16x32_bf16 v[160:163], v[48:51], v[184:187], v[160:163]\n"
    "\ts_waitcnt vmcnt(4)\n"
    "\tv_mfma_f32_16x16x32_bf16 v[160:163], v[48:51], v[188:191], v[160:163]\n"
    "\ts_waitcnt vmcnt(3)\n"
    "\tv_mfma_f32_16x16x32_bf16 v[160:163], v[48:51], v[192:195], v[160:163]\n"
    "\ts_waitcnt vmcnt(2)\n"
    "\tv_mfma_f32_16x16x32_bf16 v[160:163], v[48:51], v[196:199], v[160:163]\n"
    "\ts_waitcnt vmcnt(1)\n"
    "\tv_mfma_f32_16x16x32_bf16 v[160:163], v[48:51], v[200:203], v[160:163]\n"
    "\ts_waitcnt vmcnt(0)\n"
    "\tv_mfma_f32_16x16x32_bf16 v[160:163], v[48:51], v[204:207], v[160:163]\n"
    "\ts_nop 7\n"
    "\tv_cmp_u_f32 vcc, v160, v160\n"
    "\ts_cbranch_vccnz L%=_ps26\n"
    "L%=_pd25:\n"
    "\ts_nop 4\n"
    "\tv_cvt_pk_bf16_f32 v160, v160, v161\n"
    "\tds_write_b32 v24, v160\n"
    "\ts_mov_b32 s52, 1\n"
    "\ts_mov_b32 s50, 655360\n"
    "\ts_mov_b32 s79, 663552\n"
    "\ts_cmp_eq_u32 s48, 3\n"
    "\ts_cbranch_scc1 L%=_q2\n"
    "\ts_waitcnt lgkmcnt(0)\n"
    "\ts_barrier\n"
    "\ts_cmp_ge_u32 s27, 28\n"
    "\ts_cbranch_scc1 L%=_b_next\n"
    "\ts_cmp_eq_u32 s48, 0\n"
    "\ts_cbranch_scc1 L%=_as_qa\n"
    "\ts_barrier\n"
    "\ts_barrier\n"
    "\ts_sleep 21\n"
    "\ts_nop 0\n"
    "\ts_branch L%=_as_hb\n"
    "L%=_as_qa:\n"
    "\ts_mov_b32 s73, 0\n"
    "\ts_sleep 12\n"
    "\ts_nop 7\n"
    "\tbuffer_load_dwordx4 v[176:179], v36, s[36:39], s51 offen offset:0 sc1\n"
    "\tbuffer_load_dwordx4 v[180:183], v36, s[36:39], s51 offen offset:1024 sc1\n"
    "\tbuffer_load_dwordx4 v[184:187], v36, s[36:39], s51 offen offset:2048 sc1\n"
    "\tbuffer_load_dwordx4 v[188:191], v36, s[36:39], s51 offen offset:3072 sc1\n"
    "\ts_waitcnt vmcnt(3)\n"
    "\tv_mfma_f32_16x16x32_bf16 v[192:195], v[48:51], v[176:179], 0\n"
    "\ts_waitcnt vmcnt(2)\n"
    "\tv_mfma_f32_16x16x32_bf16 v[196:199], v[48:51], v[180:183], 0\n"
    "\ts_waitcnt vmcnt(1)\n"
    "\tv_mfma_f32_16x16x32_bf16 v[200:203], v[48:51], v[184:187], 0\n"
    "\ts_waitcnt vmcnt(0)\n"
    "\tv_mfma_f32_16x16x32_bf16 v[204:207], v[48:51], v[188:191], 0\n"
    "\ts_nop 3\n"
    "\tv_cmp_u_f32 s[62:63], v192, v196\n"
    "\ts_nop 2\n"
    "\tv_cmp_u_f32 vcc, v200, v204\n"
    "\ts_nop 0\n"
    "\ts_or_b64 vcc, vcc, s[62:63]\n"
    "\ts_cbranch_vccnz L%=_ps29\n"
    "L%=_pd28:\n"
    "\tv_cvt_pk_bf16_f32 v192, v192, v193\n"
    "\tds_write_b32 v37, v192 offset:0\n"
    "\tv_cvt_pk_bf16_f32 v196, v196, v197\n"
    "\tds_write_b32 v37, v196 offset:256\n"
    "\tv_cvt_pk_bf16_f32 v200, v200, v201\n"
    "\tds_write_b32 v37, v200 offset:640\n"
    "\tv_cvt_pk_bf16_f32 v204, v204, v205\n"
    "\tds_write_b32 v37, v204 offset:896\n"
    "\ts_waitcnt lgkmcnt(0)\n"
    "\ts_barrier\n"
    "\ts_barrier\n"
    "\ts_branch L%=_as_wo\n"
    "L%=_q2:\n"
    "\tv_mov_b64 v[160:161], 0\n"
    "\tv_mov_b64 v[162:163], 0\n"
    "\ts_mov_b32 s60, 8192\n"
    "\ts_waitcnt lgkmcnt(0)\n"
    "\ts_barrier\n"
    "\tds_read_b128 v[152:155], v21\n"
    "\tds_read_b128 v[120:123], v40 offset:0\n"
    "\tds_read_b128 v[124:127], v40 offset:128\n"
    "\tds_read_b128 v[128:131], v41 offset:0\n"
    "\tds_read_b128 v[132:135], v41 offset:128\n"
    "\tds_read_b128 v[136:139], v42 offset:0\n"
    "\tds_read_b128 v[140:143], v42 offset:128\n"
    "\tds_read_b128 v[144:147], v43 offset:0\n"
    "\tds_read_b128 v[148:151], v43 offset:128\n"
    "\ts_mov_b32 s56, 0x3f803f80\n"
    "\tv_mov_b32 v158, 0x3b124925\n"
    "\ts_waitcnt lgkmcnt(8)\n"
    "\tv_dot2_f32_bf16 v156, v152, s56, 0\n"
    "\tv_dot2_f32_bf16 v157, v152, v152, 0\n"
    "\tv_dot2_f32_bf16 v156, v153, s56, v156\n"
    "\tv_dot2_f32_bf16 v157, v153, v153, v157\n"
    "\tv_dot2_f32_bf16 v156, v154, s56, v156\n"
    "\tv_dot2_f32_bf16 v157, v154, v154, v157\n"
    "\tv_dot2_f32_bf16 v156, v155, s56, v156\n"
    "\tv_dot2_f32_bf16 v157, v155, v155, v157\n"
    "\ts_waitcnt lgkmcnt(7)\n"
    "\tv_smfmac_f32_16x16x64_bf16 v[160:163], v[120:123], v[56:63], v22\n"
    "\tv_add_f32_dpp v156, v156, v156 quad_perm:[1,0,3,2] row_mask:0xf bank_mask:0xf\n"
    "\tv_add_f32_dpp v157, v157, v157 quad_perm:[1,0,3,2] row_mask:0xf bank_mask:0xf\n"
    "\ts_waitcnt lgkmcnt(6)\n"
    "\tv_smfmac_f32_16x16x64_bf16 v[160:163], v[124:127], v[64:71], v22\n"
    "\tv_add_f32_dpp v156, v156, v156 quad_perm:[2,3,0,1] row_mask:0xf bank_mask:0xf\n"
    "\tv_add_f32_dpp v157, v157, v157 quad_perm:[2,3,0,1] row_mask:0xf bank_mask:0xf\n"
    "\ts_waitcnt lgkmcnt(5)\n"
    "\tv_smfmac_f32_16x16x64_bf16 v[160:163], v[128:131], v[72:79], v22\n"
    "\tv_add_f32_dpp v156, v156, v156 row_half_mirror row_mask:0xf bank_mask:0xf\n"
    "\tv_add_f32_dpp v157, v157, v157 row_half_mirror row_mask:0xf bank_mask:0xf\n"
    "\ts_waitcnt lgkmcnt(4)\n"
    "\tv_smfmac_f32_16x16x64_bf16 v[160:163], v[132:135], v[80:87], v22\n"
    "\tv_add_f32_dpp v156, v156, v156 row_mirror row_mask:0xf bank_mask:0xf\n"
    "\tv_add_f32_dpp v157, v157, v157 row_mirror row_mask:0xf bank_mask:0xf\n"
    "\ts_waitcnt lgkmcnt(3)\n"
    "\tv_smfmac_f32_16x16x64_bf16 v[160:163], v[136:139], v[88:95], v22\n"
    "\tv_add_f32_dpp v156, v156, v156 row_bcast:15 row_mask:0xa bank_mask:0xf\n"
    "\tv_add_f32_dpp v157, v157, v157 row_bcast:15 row_mask:0xa bank_mask:0xf\n"
    "\ts_waitcnt lgkmcnt(2)\n"
    "\tv_smfmac_f32_16x16x64_bf16 v[160:163], v[140:143], v[96:103], v22\n"
    "\tv_add_f32_dpp v156, v156, v156 row_bcast:31 row_mask:0xc bank_mask:0xf\n"
    "\tv_add_f32_dpp v157, v157, v157 row_bcast:31 row_mask:0xc bank_mask:0xf\n"
    "\ts_waitcnt lgkmcnt(1)\n"
    "\tv_smfmac_f32_16x16x64_bf16 v[160:163], v[144:147], v[104:111], v22\n"
    "\tv_readlane_b32 s58, v156, 63\n"
    "\tv_readlane_b32 s59, v157, 63\n"
    "\ts_waitcnt lgkmcnt(0)\n"
    "\tv_smfmac_f32_16x16x64_bf16 v[160:163], v[148:151], v[112:119], v22\n"
    "\tv_mul_f32 v152, s58, v158\n"
    "\tv_mul_f32 v153, s59, v158\n"
    "\tv_fma_f32 v153, -v152, v152, v153\n"
    "\tv_max_f32 v153, 0, v153\n"
    "\tv_add_f32 v153, 0x3727c5ac, v153\n"
    "\tv_rsq_f32 v153, v153\n"
    "\ts_nop 1\n"
    "\tv_add_f32 v158, v160, v161\n"
    "\tv_fma_f32 v158, -v152, v23, v158\n"
    "\tv_mul_f32 v158, v158, v153\n"
    "\ts_nop 1\n"
    "\tv_mov_b32_dpp v159, v158 quad_perm:[1,0,3,2] row_mask:0xf bank_mask:0xf\n"
    "\tv_cvt_pk_bf16_f32 v158, v158, v159\n"
    "\tbuffer_store_dword v158, v34, s[36:39], 0 offen sc1\n"
    "\tbuffer_store_dword v158, v34, s[36:39], s60 offen sc1\n"
    "\ts_cmp_ge_u32 s27, 28\n"
    "\ts_cbranch_scc1 L%=_b_next\n"
    "\ts_barrier\n"
    "\ts_barrier\n"
    "\ts_barrier\n"
    "\ts_endpgm\n"
    "L%=_b_next:\n"
    "\ts_cmp_ge_u32 s54, 28\n"
    "\ts_cbranch_scc1 L%=_exit\n"
    "\ts_cmp_lg_u32 s48, 0\n"
    "\ts_cbranch_scc1 L%=_b_res\n"
    "\ts_sleep 87\n"
    "\ts_nop 7\n"
    "\ts_branch L%=_b_res\n"
    "L%=_b_svc:\n"
    "\ts_cmp_ge_u32 s54, 28\n"
    "\ts_cbranch_scc1 L%=_b_nodma\n"
    "\ts_mov_b32 s40, s18\n"
    "\ts_and_b32 s41, s19, 0xffff\n"
    "\ts_mov_b32 s42, 1605632\n"
    "\ts_mov_b32 s43, 0x20000\n"
    "\ts_mov_b32 s44, s20\n"
    "\ts_and_b32 s45, s21, 0xffff\n"
    "\ts_mov_b32 s46, 1605632\n"
    "\ts_mov_b32 s47, 0x20000\n"
    "\tv_lshrrev_b32 v3, 3, v1\n"
    "\tv_mul_u32_u24 v8, 3584, v3\n"
    "\tv_lshlrev_b32 v7, 1, v5\n"
    "\tv_and_b32 v3, 7, v1\n"
    "\tv_xor_b32 v3, v3, v7\n"
    "\tv_lshlrev_b32 v10, 4, v3\n"
    "\tv_lshlrev_b32 v9, 4, v1\n"
    "\ts_mul_i32 s56, s5, 7\n"
    "\ts_lshl_b32 m0, s56, 10\n"
    "\ts_mul_i32 s57, s5, 200704\n"
    "\ts_lshl_b32 s58, s54, 7\n"
    "\ts_add_u32 s57, s57, s58\n"
    "\ts_add_u32 s60, s74, 8000\n"
    "L%=_wc30:\n"
    "\ts_sleep 2\n"
    "\ts_memtime s[64:65]\n"
    "\ts_waitcnt lgkmcnt(0)\n"
    "\ts_sub_u32 s61, s64, s60\n"
    "\ts_cmp_lt_i32 s61, 0\n"
    "\ts_cbranch_scc1 L%=_wc30\n"
    "\ts_and_b32 s58, s56, 2\n"
    "\ts_lshl_b32 s58, s58, 5\n"
    "\tv_xor_b32 v3, s58, v10\n"
    "\tv_add_u32 v3, v3, v8\n"
    "\tbuffer_load_dwordx4 v3, s[40:43], s57 offen nt lds\n"
    "\ts_waitcnt vmcnt(3)\n"
    "\ts_add_u32 m0, m0, 1024\n"
    "\ts_add_u32 s57, s57, 28672\n"
    "\ts_add_u32 s56, s56, 1\n"
    "\ts_and_b32 s58, s56, 2\n"
    "\ts_lshl_b32 s58, s58, 5\n"
    "\tv_xor_b32 v3, s58, v10\n"
    "\tv_add_u32 v3, v3, v8\n"
    "\tbuffer_load_dwordx4 v3, s[40:43], s57 offen nt lds\n"
    "\ts_waitcnt vmcnt(3)\n"
    "\ts_add_u32 m0, m0, 1024\n"
    "\ts_add_u32 s57, s57, 28672\n"
    "\ts_add_u32 s56, s56, 1\n"
    "\ts_and_b32 s58, s56, 2\n"
    "\ts_lshl_b32 s58, s58, 5\n"
    "\tv_xor_b32 v3, s58, v10\n"
    "\tv_add_u32 v3, v3, v8\n"
    "\tbuffer_load_dwordx4 v3, s[40:43], s57 offen nt lds\n"
    "\ts_waitcnt vmcnt(3)\n"
    "\ts_add_u32 m0, m0, 1024\n"
    "\ts_add_u32 s57, s57, 28672\n"
    "\ts_add_u32 s56, s56, 1\n"
    "\ts_and_b32 s58, s56, 2\n"
    "\ts_lshl_b32 s58, s58, 5\n"
    "\tv_xor_b32 v3, s58, v10\n"
    "\tv_add_u32 v3, v3, v8\n"
    "\tbuffer_load_dwordx4 v3, s[40:43], s57 offen nt lds\n"
    "\ts_waitcnt vmcnt(3)\n"
    "\ts_add_u32 m0, m0, 1024\n"
    "\ts_add_u32 s57, s57, 28672\n"
    "\ts_add_u32 s56, s56, 1\n"
    "\ts_and_b32 s58, s56, 2\n"
    "\ts_lshl_b32 s58, s58, 5\n"
    "\tv_xor_b32 v3, s58, v10\n"
    "\tv_add_u32 v3, v3, v8\n"
    "\tbuffer_load_dwordx4 v3, s[40:43], s57 offen nt lds\n"
    "\ts_waitcnt vmcnt(3)\n"
    "\ts_add_u32 m0, m0, 1024\n"
    "\ts_add_u32 s57, s57, 28672\n"
    "\ts_add_u32 s56, s56, 1\n"
    "\ts_and_b32 s58, s56, 2\n"
    "\ts_lshl_b32 s58, s58, 5\n"
    "\tv_xor_b32 v3, s58, v10\n"
    "\tv_add_u32 v3, v3, v8\n"
    "\tbuffer_load_dwordx4 v3, s[40:43], s57 offen nt lds\n"
    "\ts_waitcnt vmcnt(3)\n"
    "\ts_add_u32 m0, m0, 1024\n"
    "\ts_add_u32 s57, s57, 28672\n"
    "\ts_add_u32 s56, s56, 1\n"
    "\ts_and_b32 s58, s56, 2\n"
    "\ts_lshl_b32 s58, s58, 5\n"
    "\tv_xor_b32 v3, s58, v10\n"
    "\tv_add_u32 v3, v3, v8\n"
    "\tbuffer_load_dwordx4 v3, s[40:43], s57 offen nt lds\n"
    "\ts_waitcnt vmcnt(3)\n"
    "\ts_mul_i32 s56, s5, 7424\n"
    "\ts_add_u32 m0, s56, 57344\n"
    "\ts_mul_i32 s58, s54, 57344\n"
    "\ts_mul_i32 s56, s5, 7168\n"
    "\ts_add_u32 s58, s58, s56\n"
    "\ts_mov_b32 exec_hi, 0xffffff\n"
    "\tbuffer_load_dwordx4 v9, s[44:47], s58 offen nt lds\n"
    "\ts_waitcnt vmcnt(3)\n"
    "\ts_add_u32 m0, m0, 928\n"
    "\ts_add_u32 s58, s58, 896\n"
    "\tbuffer_load_dwordx4 v9, s[44:47], s58 offen nt lds\n"
    "\ts_waitcnt vmcnt(3)\n"
    "\ts_add_u32 m0, m0, 928\n"
    "\ts_add_u32 s58, s58, 896\n"
    "\tbuffer_load_dwordx4 v9, s[44:47], s58 offen nt lds\n"
    "\ts_waitcnt vmcnt(3)\n"
    "\ts_add_u32 m0, m0, 928\n"
    "\ts_add_u32 s58, s58, 896\n"
    "\tbuffer_load_dwordx4 v9, s[44:47], s58 offen nt lds\n"
    "\ts_waitcnt vmcnt(3)\n"
    "\ts_add_u32 m0, m0, 928\n"
    "\ts_add_u32 s58, s58, 896\n"
    "\tbuffer_load_dwordx4 v9, s[44:47], s58 offen nt lds\n"
    "\ts_waitcnt vmcnt(3)\n"
    "\ts_add_u32 m0, m0, 928\n"
    "\ts_add_u32 s58, s58, 896\n"
    "\tbuffer_load_dwordx4 v9, s[44:47], s58 offen nt lds\n"
    "\ts_waitcnt vmcnt(3)\n"
    "\ts_add_u32 m0, m0, 928\n"
    "\ts_add_u32 s58, s58, 896\n"
    "\tbuffer_load_dwordx4 v9, s[44:47], s58 offen nt lds\n"
    "\ts_waitcnt vmcnt(3)\n"
    "\ts_add_u32 m0, m0, 928\n"
    "\ts_add_u32 s58, s58, 896\n"
    "\tbuffer_load_dwordx4 v9, s[44:47], s58 offen nt lds\n"
    "\ts_waitcnt vmcnt(3)\n"
    "\ts_mov_b32 exec_hi, -1\n"
    "L%=_b_nodma:\n"
    "\tv_mov_b32 v3, 161028\n"
    "\tv_bfrev_b32 v7, 1\n"
    "\tds_write_b32 v3, v7\n"
    "\ts_cmp_lg_u32 s48, 1\n"
    "\ts_cbranch_scc1 L%=_b_na\n"
    "\ts_add_u32 s60, s74, 11500\n"
    "L%=_wc31:\n"
    "\ts_sleep 2\n"
    "\ts_memtime s[64:65]\n"
    "\ts_waitcnt lgkmcnt(0)\n"
    "\ts_sub_u32 s61, s64, s60\n"
    "\ts_cmp_lt_i32 s61, 0\n"
    "\ts_cbranch_scc1 L%=_wc31\n"
    "\tv_lshlrev_b32 v153, 7, v6\n"
    "\tv_add_u32 v153, 1556480, v153\n"
    "\ts_mov_b32 s73, 0\n"
    "\tbuffer_load_dwordx4 v[176:179], v153, s[36:39], 0 offen sc1\n"
    "L%=_b_ar:\n"
    "\ts_add_u32 s73, s73, 1\n"
    "\ts_waitcnt vmcnt(0)\n"
    "\tv_xor_b32 v154, s70, v176\n"
    "\tv_xor_b32 v155, s71, v177\n"
    "\tv_xor_b32 v156, s71, v179\n"
    "\tv_or3_b32 v154, v154, v155, v156\n"
    "\tv_cmp_ne_u32 vcc, 0, v154\n"
    "\ts_cbranch_vccz L%=_b_arok\n"
    "\ts_cmp_ge_u32 s73, 40000\n"
    "\ts_cbranch_scc1 L%=_b_arok\n"
    "\ts_sleep 4\n"
    "\tbuffer_load_dwordx4 v[176:179], v153, s[36:39], 0 offen sc1\n"
    "\ts_branch L%=_b_ar\n"
    "L%=_b_arok:\n"
    "\tv_xor_b32 v184, s70, v178\n"
    "\ts_nop 1\n"
    "\tv_add_u32_dpp v184, v184, v184 quad_perm:[1,0,3,2] row_mask:0xf bank_mask:0xf\n"
    "\ts_nop 1\n"
    "\tv_add_u32_dpp v184, v184, v184 quad_perm:[2,3,0,1] row_mask:0xf bank_mask:0xf\n"
    "\ts_nop 1\n"
    "\tv_add_u32_dpp v184, v184, v184 row_half_mirror row_mask:0xf bank_mask:0xf\n"
    "\ts_nop 1\n"
    "\tv_add_u32_dpp v184, v184, v184 row_mirror row_mask:0xf bank_mask:0xf\n"
    "\ts_nop 1\n"
    "\tv_add_u32_dpp v184, v184, v184 row_bcast:15 row_mask:0xa bank_mask:0xf\n"
    "\ts_nop 1\n"
    "\tv_add_u32_dpp v184, v184, v184 row_bcast:31 row_mask:0xc bank_mask:0xf\n"
    "\ts_nop 0\n"
    "\tv_readlane_b32 s60, v184, 63\n"
    "\ts_nop 0\n"
    "\ts_cmp_gt_i32 s60, 0\n"
    "\ts_cselect_b32 s60, 1, 0\n"
    "\ts_xor_b32 s60, s60, s78\n"
    "\ts_lshl_b32 s60, s60, 13\n"
    "\tv_mov_b32 v155, s60\n"
    "\tv_mov_b32 v3, 161024\n"
    "\tds_write_b32 v3, v155\n"
    "L%=_b_na:\n"
    "\ts_waitcnt vmcnt(0)\n"
    "\ts_waitcnt lgkmcnt(0)\n"
    "\ts_barrier\n"
    "\tv_mov_b32 v3, 161024\n"
    "\tds_read_b32 v3, v3\n"
    "\ts_waitcnt lgkmcnt(0)\n"
    "\tv_readfirstlane_b32 s51, v3\n"
    "\ts_cmp_lt_u32 s54, 28\n"
    "\ts_cbranch_scc1 L%=_b_pre\n"
    "\ts_cmp_eq_u32 s48, 1\n"
    "\ts_cbranch_scc1 L%=_as_w1\n"
    "\ts_branch L%=_as_bt\n"
    "L%=_b_pre:\n"
    "\ts_cmp_eq_u32 s54, 0\n"
    "\ts_cselect_b32 s56, 643072, 1073741824\n"
    "\ts_lshl_b32 s57, s48, 8\n"
    "\ts_add_u32 s56, s56, s57\n"
    "\ts_cmp_eq_u32 s48, 3\n"
    "\ts_cselect_b32 s57, 8, 16\n"
    "\ts_cselect_b32 s58, 31, 63\n"
    "\tv_lshlrev_b32 v30, 4, v2\n"
    "\tv_lshl_add_u32 v30, v4, 2, v30\n"
    "\tv_add_u32 v30, s56, v30\n"
    "\tv_mov_b32 v7, 1073741824\n"
    "\tv_cmp_gt_u32 vcc, s57, v2\n"
    "\ts_nop 1\n"
    "\tv_cndmask_b32 v30, v7, v30, vcc\n"
    "\ts_lshl_b32 s56, s48, 12\n"
    "\ts_lshr_b32 s61, s56, 13\n"
    "\ts_lshl_b32 s61, s61, 13\n"
    "\ts_add_u32 s56, s56, s61\n"
    "\ts_add_u32 s56, s56, 1376256\n"
    "\tv_and_b32 v153, s58, v1\n"
    "\tv_lshlrev_b32 v153, 6, v153\n"
    "\tv_add_u32 v153, s56, v153\n"
    "\ts_memrealtime s[62:63]\n"
    "\ts_mov_b32 s73, 0\n"
    "\tv_mov_b32 v154, 161028\n"
    "\ts_waitcnt lgkmcnt(0)\n"
    "\ts_lshr_b32 s75, s62, 20\n"
    "\ts_lshl_b32 s75, s75, 20\n"
    "L%=_b_hb:\n"
    "\tbuffer_load_dword v152, v153, s[36:39], s51 offen sc1\n"
    "\ts_add_u32 s73, s73, 1\n"
    "\ts_waitcnt vmcnt(0)\n"
    "\tv_cmp_eq_u32 vcc, -1, v152\n"
    "\ts_cbranch_vccz L%=_b_hbok\n"
    "\ts_cmp_lt_u32 s73, 40000\n"
    "\ts_cbranch_scc1 L%=_b_hb\n"
    "L%=_b_hbok:\n"
    "\tv_subrev_u32 v152, s75, v152\n"
    "\tv_bfe_i32 v152, v152, 0, 31\n"
    "\tds_max_i32 v154, v152\n"
    "\ts_waitcnt lgkmcnt(0)\n"
    "\ts_barrier\n"
    "\ts_cmp_lg_u32 s48, 0\n"
    "\ts_cbranch_scc1 L%=_b_res\n"
    "\ts_lshl_b32 s56, s54, 6\n"
    "\ts_add_u32 s56, s56, 1409024\n"
    "\tds_read_b32 v152, v154\n"
    "\tv_mov_b32 v153, s56\n"
    "\tv_mov_b32 v7, 1073741824\n"
    "\tv_cmp_eq_u32 vcc, 0, v1\n"
    "\ts_nop 1\n"
    "\tv_cndmask_b32 v153, v7, v153, vcc\n"
    "\ts_waitcnt lgkmcnt(0)\n"
    "\tv_add_u32 v152, s75, v152\n"
    "\tv_and_b32 v152, 0x7fffffff, v152\n"
    "\ts_nop 0\n"
    "\tv_readfirstlane_b32 s75, v152\n"
    "\tbuffer_store_dword v152, v153, s[36:39], s50 offen sc1\n"
    "\tbuffer_store_dword v152, v153, s[36:39], s79 offen sc1\n"
    "\ts_memrealtime s[62:63]\n"
    "\ts_memtime s[64:65]\n"
    "\ts_add_u32 s60, s75, 140\n"
    "\ts_waitcnt lgkmcnt(0)\n"
    "\ts_sub_u32 s60, s60, s62\n"
    "\ts_bfe_i32 s60, s60, 2031616\n"
    "\ts_min_i32 s60, s60, 2000\n"
    "\ts_max_i32 s60, s60, 0\n"
    "\ts_mul_i32 s60, s60, 24\n"
    "\ts_add_u32 s60, s60, s64\n"
    "L%=_wc32:\n"
    "\ts_memtime s[64:65]\n"
    "\ts_waitcnt lgkmcnt(0)\n"
    "\ts_sub_u32 s61, s64, s60\n"
    "\ts_cmp_lt_i32 s61, 0\n"
    "\ts_cbranch_scc1 L%=_wc32\n"
    "L%=_b_res:\n"
    "\tds_read_b32 v52, v24\n"
    "\tv_mov_b64 v[54:55], 0\n"
    "\ts_add_u32 s56, s50, s51\n"
    "\ts_add_u32 s57, s56, 573440\n"
    "\ts_add_u32 s56, s56, 557056\n"
    "\ts_mov_b32 s73, 0\n"
    "\ts_waitcnt lgkmcnt(0)\n"
    "\tv_and_b32 v53, 0xffff0000, v52\n"
    "\tv_lshlrev_b32 v52, 16, v52\n"
    "\ts_barrier\n"
    "\tbuffer_load_dwordx4 v[176:179], v26, s[36:39], s56 offen sc1\n"
    "\tbuffer_load_dwordx4 v[180:183], v27, s[36:39], s56 offen sc1\n"
    "\tbuffer_load_dwordx4 v[184:187], v26, s[36:39], s57 offen sc1\n"
    "\tbuffer_load_dwordx4 v[188:191], v27, s[36:39], s57 offen sc1\n"
    "\ts_waitcnt vmcnt(3)\n"
    "\tv_mfma_f32_16x16x32_bf16 v[160:163], v[48:51], v[176:179], v[52:55]\n"
    "\ts_waitcnt vmcnt(2)\n"
    "\tv_mfma_f32_16x16x32_bf16 v[160:163], v[48:51], v[180:183], v[160:163]\n"
    "\ts_waitcnt vmcnt(1)\n"
    "\tv_mfma_f32_16x16x32_bf16 v[160:163], v[48:51], v[184:187], v[160:163]\n"
    "\ts_waitcnt vmcnt(0)\n"
    "\tv_mfma_f32_16x16x32_bf16 v[160:163], v[48:51], v[188:191], v[160:163]\n"
    "\ts_nop 7\n"
    "\tv_cmp_u_f32 vcc, v160, v160\n"
    "\ts_cbranch_vccnz L%=_ps35\n"
    "L%=_pd34:\n"
    "\ts_nop 4\n"
    "\tv_cvt_pk_bf16_f32 v160, v160, v161\n"
    "\tds_write_b32 v24, v160\n"
    "\ts_waitcnt lgkmcnt(0)\n"
    "\ts_barrier\n"
    "\tbuffer_store_dword v160, v30, s[36:39], s50 offen sc1\n"
    "\tbuffer_store_dword v160, v30, s[36:39], s79 offen sc1\n"
    "\ts_barrier\n"
    "\ts_cmp_lg_u32 s52, 0\n"
    "\ts_cbranch_scc1 L%=_exit\n"
    "\ts_cmp_lg_u32 s48, 0\n"
    "\ts_cbranch_scc1 L%=_as_bt\n"
    "\ts_memrealtime s[62:63]\n"
    "\ts_memtime s[64:65]\n"
    "\ts_add_u32 s60, s75, 240\n"
    "\ts_waitcnt lgkmcnt(0)\n"
    "\ts_sub_u32 s60, s60, s62\n"
    "\ts_bfe_i32 s60, s60, 2031616\n"
    "\ts_min_i32 s60, s60, 2000\n"
    "\ts_max_i32 s60, s60, 0\n"
    "\ts_mul_i32 s60, s60, 24\n"
    "\ts_add_u32 s60, s60, s64\n"
    "L%=_wc36:\n"
    "\ts_memtime s[64:65]\n"
    "\ts_waitcnt lgkmcnt(0)\n"
    "\ts_sub_u32 s61, s64, s60\n"
    "\ts_cmp_lt_i32 s61, 0\n"
    "\ts_cbranch_scc1 L%=_wc36\n"
    "\ts_branch L%=_as_bt\n"
    "L%=_ps19:\n"
    "\ts_mov_b64 s[62:63], 0\n"
    "\tv_max_u32 v156, v176, v177\n"
    "\tv_cmp_eq_u32 vcc, -1, v156\n"
    "\ts_cbranch_vccz L%=_rp37\n"
    "\ts_or_b64 s[62:63], s[62:63], vcc\n"
    "\ts_mov_b64 exec, vcc\n"
    "\tbuffer_load_dwordx2 v[176:177], v29, s[36:39], s56 offen sc1\n"
    "\ts_mov_b64 exec, -1\n"
    "L%=_rp37:\n"
    "\tv_max3_u32 v156, v180, v181, v182\n"
    "\tv_max_u32 v156, v156, v183\n"
    "\tv_cmp_eq_u32 vcc, -1, v156\n"
    "\ts_cbranch_vccz L%=_rp38\n"
    "\ts_or_b64 s[62:63], s[62:63], vcc\n"
    "\ts_mov_b64 exec, vcc\n"
    "\tbuffer_load_dwordx4 v[180:183], v30, s[36:39], s56 offen offset:0 sc1\n"
    "\ts_mov_b64 exec, -1\n"
    "L%=_rp38:\n"
    "\tv_max3_u32 v156, v184, v185, v186\n"
    "\tv_max_u32 v156, v156, v187\n"
    "\tv_cmp_eq_u32 vcc, -1, v156\n"
    "\ts_cbranch_vccz L%=_rp39\n"
    "\ts_or_b64 s[62:63], s[62:63], vcc\n"
    "\ts_mov_b64 exec, vcc\n"
    "\tbuffer_load_dwordx4 v[184:187], v30, s[36:39], s56 offen offset:2048 sc1\n"
    "\ts_mov_b64 exec, -1\n"
    "L%=_rp39:\n"
    "\tv_max3_u32 v156, v188, v189, v190\n"
    "\tv_max_u32 v156, v156, v191\n"
    "\tv_cmp_eq_u32 vcc, -1, v156\n"
    "\ts_cbranch_vccz L%=_rp40\n"
    "\ts_or_b64 s[62:63], s[62:63], vcc\n"
    "\ts_mov_b64 exec, vcc\n"
    "\tbuffer_load_dwordx4 v[188:191], v30, s[36:39], s57 offen offset:0 sc1\n"
    "\ts_mov_b64 exec, -1\n"
    "L%=_rp40:\n"
    "\tv_max3_u32 v156, v192, v193, v194\n"
    "\tv_max_u32 v156, v156, v195\n"
    "\tv_cmp_eq_u32 vcc, -1, v156\n"
    "\ts_cbranch_vccz L%=_rp41\n"
    "\ts_or_b64 s[62:63], s[62:63], vcc\n"
    "\ts_mov_b64 exec, vcc\n"
    "\tbuffer_load_dwordx4 v[192:195], v30, s[36:39], s57 offen offset:2048 sc1\n"
    "\ts_mov_b64 exec, -1\n"
    "L%=_rp41:\n"
    "\tv_max3_u32 v156, v196, v197, v198\n"
    "\tv_max_u32 v156, v156, v199\n"
    "\tv_cmp_eq_u32 vcc, -1, v156\n"
    "\ts_cbranch_vccz L%=_rp42\n"
    "\ts_or_b64 s[62:63], s[62:63], vcc\n"
    "\ts_mov_b64 exec, vcc\n"
    "\tbuffer_load_dwordx4 v[196:199], v30, s[36:39], s58 offen offset:0 sc1\n"
    "\ts_mov_b64 exec, -1\n"
    "L%=_rp42:\n"
    "\tv_max3_u32 v156, v200, v201, v202\n"
    "\tv_max_u32 v156, v156, v203\n"
    "\tv_cmp_eq_u32 vcc, -1, v156\n"
    "\ts_cbranch_vccz L%=_rp43\n"
    "\ts_or_b64 s[62:63], s[62:63], vcc\n"
    "\ts_mov_b64 exec, vcc\n"
    "\tbuffer_load_dwordx4 v[200:203], v30, s[36:39], s58 offen offset:2048 sc1\n"
    "\ts_mov_b64 exec, -1\n"
    "L%=_rp43:\n"
    "\tv_max3_u32 v156, v204, v205, v206\n"
    "\tv_max_u32 v156, v156, v207\n"
    "\tv_cmp_eq_u32 vcc, -1, v156\n"
    "\ts_cbranch_vccz L%=_rp44\n"
    "\ts_or_b64 s[62:63], s[62:63], vcc\n"
    "\ts_mov_b64 exec, vcc\n"
    "\tbuffer_load_dwordx4 v[204:207], v30, s[36:39], s59 offen offset:0 sc1\n"
    "\ts_mov_b64 exec, -1\n"
    "L%=_rp44:\n"
    "\ts_add_u32 s73, s73, 1\n"
    "\ts_cmp_lg_u64 s[62:63], 0\n"
    "\ts_cselect_b32 s61, s73, 40000\n"
    "\ts_waitcnt vmcnt(0)\n"
    "\ts_cmp_lt_u32 s61, 40000\n"
    "\ts_cbranch_scc1 L%=_pa17\n"
    "\ts_branch L%=_pd18\n"
    "L%=_pa17:\n"
    "\tv_mov_b32 v152, 0xff800000\n"
    "\tv_cndmask_b32 v153, v152, v176, s[66:67]\n"
    "\tv_cndmask_b32 v152, v152, v176, s[66:67]\n"
    "\ts_nop 1\n"
    "\tv_max_f32_dpp v152, v152, v152 quad_perm:[1,0,3,2] row_mask:0xf bank_mask:0xf\n"
    "\ts_nop 1\n"
    "\tv_max_f32_dpp v152, v152, v152 quad_perm:[2,3,0,1] row_mask:0xf bank_mask:0xf\n"
    "\ts_nop 1\n"
    "\tv_max_f32_dpp v152, v152, v152 row_half_mirror row_mask:0xf bank_mask:0xf\n"
    "\ts_nop 1\n"
    "\tv_max_f32_dpp v152, v152, v152 row_mirror row_mask:0xf bank_mask:0xf\n"
    "\ts_nop 1\n"
    "\tv_max_f32_dpp v152, v152, v152 row_bcast:15 row_mask:0xa bank_mask:0xf\n"
    "\ts_nop 1\n"
    "\tv_max_f32_dpp v152, v152, v152 row_bcast:31 row_mask:0xc bank_mask:0xf\n"
    "\ts_nop 0\n"
    "\tv_readlane_b32 s60, v152, 63\n"
    "\ts_nop 1\n"
    "\tv_subrev_f32 v153, s60, v153\n"
    "\tv_exp_f32 v153, v153\n"
    "\ts_nop 0\n"
    "\tv_cvt_pk_bf16_f32 v154, v153, v153\n"
    "\tv_mul_f32 v155, v153, v177\n"
    "\ts_nop 0\n"
    "\tv_and_b32_dpp v208, v154, v44 row_newbcast:0 row_mask:0xf bank_mask:0xf\n"
    "\tv_and_b32_dpp v209, v154, v45 row_newbcast:0 row_mask:0xf bank_mask:0xf\n"
    "\tv_and_b32_dpp v210, v154, v46 row_newbcast:0 row_mask:0xf bank_mask:0xf\n"
    "\tv_and_b32_dpp v211, v154, v47 row_newbcast:0 row_mask:0xf bank_mask:0xf\n"
    "\tv_add_f32_dpp v155, v155, v155 quad_perm:[1,0,3,2] row_mask:0xf bank_mask:0xf\n"
    "\ts_nop 0\n"
    "\tv_mfma_f32_16x16x32_bf16 v[160:163], v[208:211], v[180:183], 0\n"
    "\tv_and_b32_dpp v212, v154, v44 row_newbcast:1 row_mask:0xf bank_mask:0xf\n"
    "\tv_and_b32_dpp v213, v154, v45 row_newbcast:1 row_mask:0xf bank_mask:0xf\n"
    "\tv_and_b32_dpp v214, v154, v46 row_newbcast:1 row_mask:0xf bank_mask:0xf\n"
    "\tv_and_b32_dpp v215, v154, v47 row_newbcast:1 row_mask:0xf bank_mask:0xf\n"
    "\tv_add_f32_dpp v155, v155, v155 quad_perm:[2,3,0,1] row_mask:0xf bank_mask:0xf\n"
    "\ts_nop 0\n"
    "\tv_mfma_f32_16x16x32_bf16 v[160:163], v[212:215], v[184:187], v[160:163]\n"
    "\tv_and_b32_dpp v208, v154, v44 row_newbcast:2 row_mask:0xf bank_mask:0xf\n"
    "\tv_and_b32_dpp v209, v154, v45 row_newbcast:2 row_mask:0xf bank_mask:0xf\n"
    "\tv_and_b32_dpp v210, v154, v46 row_newbcast:2 row_mask:0xf bank_mask:0xf\n"
    "\tv_and_b32_dpp v211, v154, v47 row_newbcast:2 row_mask:0xf bank_mask:0xf\n"
    "\tv_add_f32_dpp v155, v155, v155 row_half_mirror row_mask:0xf bank_mask:0xf\n"
    "\ts_nop 0\n"
    "\tv_mfma_f32_16x16x32_bf16 v[160:163], v[208:211], v[188:191], v[160:163]\n"
    "\tv_and_b32_dpp v212, v154, v44 row_newbcast:3 row_mask:0xf bank_mask:0xf\n"
    "\tv_and_b32_dpp v213, v154, v45 row_newbcast:3 row_mask:0xf bank_mask:0xf\n"
    "\tv_and_b32_dpp v214, v154, v46 row_newbcast:3 row_mask:0xf bank_mask:0xf\n"
    "\tv_and_b32_dpp v215, v154, v47 row_newbcast:3 row_mask:0xf bank_mask:0xf\n"
    "\tv_add_f32_dpp v155, v155, v155 row_mirror row_mask:0xf bank_mask:0xf\n"
    "\ts_nop 0\n"
    "\tv_mfma_f32_16x16x32_bf16 v[160:163], v[212:215], v[192:195], v[160:163]\n"
    "\tv_and_b32_dpp v208, v154, v44 row_newbcast:4 row_mask:0xf bank_mask:0xf\n"
    "\tv_and_b32_dpp v209, v154, v45 row_newbcast:4 row_mask:0xf bank_mask:0xf\n"
    "\tv_and_b32_dpp v210, v154, v46 row_newbcast:4 row_mask:0xf bank_mask:0xf\n"
    "\tv_and_b32_dpp v211, v154, v47 row_newbcast:4 row_mask:0xf bank_mask:0xf\n"
    "\tv_add_f32_dpp v155, v155, v155 row_bcast:15 row_mask:0xa bank_mask:0xf\n"
    "\ts_nop 0\n"
    "\tv_mfma_f32_16x16x32_bf16 v[160:163], v[208:211], v[196:199], v[160:163]\n"
    "\tv_and_b32_dpp v212, v154, v44 row_newbcast:5 row_mask:0xf bank_mask:0xf\n"
    "\tv_and_b32_dpp v213, v154, v45 row_newbcast:5 row_mask:0xf bank_mask:0xf\n"
    "\tv_and_b32_dpp v214, v154, v46 row_newbcast:5 row_mask:0xf bank_mask:0xf\n"
    "\tv_and_b32_dpp v215, v154, v47 row_newbcast:5 row_mask:0xf bank_mask:0xf\n"
    "\tv_add_f32_dpp v155, v155, v155 row_bcast:31 row_mask:0xc bank_mask:0xf\n"
    "\ts_nop 0\n"
    "\tv_mfma_f32_16x16x32_bf16 v[160:163], v[212:215], v[200:203], v[160:163]\n"
    "\tv_and_b32_dpp v208, v154, v44 row_newbcast:6 row_mask:0xf bank_mask:0xf\n"
    "\tv_and_b32_dpp v209, v154, v45 row_newbcast:6 row_mask:0xf bank_mask:0xf\n"
    "\tv_and_b32_dpp v210, v154, v46 row_newbcast:6 row_mask:0xf bank_mask:0xf\n"
    "\tv_and_b32_dpp v211, v154, v47 row_newbcast:6 row_mask:0xf bank_mask:0xf\n"
    "\tv_readlane_b32 s60, v155, 63\n"
    "\ts_nop 0\n"
    "\tv_mfma_f32_16x16x32_bf16 v[160:163], v[208:211], v[204:207], v[160:163]\n"
    "\tv_rcp_f32 v155, s60\n"
    "\ts_nop 6\n"
    "\tv_cmp_u_f32 vcc, v160, v155\n"
    "\ts_cbranch_vccnz L%=_ps19\n"
    "\ts_branch L%=_pd18\n"
    "L%=_ps22:\n"
    "\ts_mov_b64 s[62:63], 0\n"
    "\tv_max3_u32 v156, v176, v177, v178\n"
    "\tv_max_u32 v156, v156, v179\n"
    "\tv_cmp_eq_u32 vcc, -1, v156\n"
    "\ts_cbranch_vccz L%=_rp45\n"
    "\ts_or_b64 s[62:63], s[62:63], vcc\n"
    "\ts_mov_b64 exec, vcc\n"
    "\tbuffer_load_dwordx4 v[176:179], v26, s[36:39], s56 offen sc1\n"
    "\ts_mov_b64 exec, -1\n"
    "L%=_rp45:\n"
    "\tv_max3_u32 v156, v180, v181, v182\n"
    "\tv_max_u32 v156, v156, v183\n"
    "\tv_cmp_eq_u32 vcc, -1, v156\n"
    "\ts_cbranch_vccz L%=_rp46\n"
    "\ts_or_b64 s[62:63], s[62:63], vcc\n"
    "\ts_mov_b64 exec, vcc\n"
    "\tbuffer_load_dwordx4 v[180:183], v27, s[36:39], s56 offen sc1\n"
    "\ts_mov_b64 exec, -1\n"
    "L%=_rp46:\n"
    "\tv_max3_u32 v156, v184, v185, v186\n"
    "\tv_max_u32 v156, v156, v187\n"
    "\tv_cmp_eq_u32 vcc, -1, v156\n"
    "\ts_cbranch_vccz L%=_rp47\n"
    "\ts_or_b64 s[62:63], s[62:63], vcc\n"
    "\ts_mov_b64 exec, vcc\n"
    "\tbuffer_load_dwordx4 v[184:187], v26, s[36:39], s57 offen sc1\n"
    "\ts_mov_b64 exec, -1\n"
    "L%=_rp47:\n"
    "\tv_max3_u32 v156, v188, v189, v190\n"
    "\tv_max_u32 v156, v156, v191\n"
    "\tv_cmp_eq_u32 vcc, -1, v156\n"
    "\ts_cbranch_vccz L%=_rp48\n"
    "\ts_or_b64 s[62:63], s[62:63], vcc\n"
    "\ts_mov_b64 exec, vcc\n"
    "\tbuffer_load_dwordx4 v[188:191], v27, s[36:39], s57 offen sc1\n"
    "\ts_mov_b64 exec, -1\n"
    "L%=_rp48:\n"
    "\tv_max3_u32 v156, v192, v193, v194\n"
    "\tv_max_u32 v156, v156, v195\n"
    "\tv_cmp_eq_u32 vcc, -1, v156\n"
    "\ts_cbranch_vccz L%=_rp49\n"
    "\ts_or_b64 s[62:63], s[62:63], vcc\n"
    "\ts_mov_b64 exec, vcc\n"
    "\tbuffer_load_dwordx4 v[192:195], v26, s[36:39], s58 offen sc1\n"
    "\ts_mov_b64 exec, -1\n"
    "L%=_rp49:\n"
    "\tv_max3_u32 v156, v196, v197, v198\n"
    "\tv_max_u32 v156, v156, v199\n"
    "\tv_cmp_eq_u32 vcc, -1, v156\n"
    "\ts_cbranch_vccz L%=_rp50\n"
    "\ts_or_b64 s[62:63], s[62:63], vcc\n"
    "\ts_mov_b64 exec, vcc\n"
    "\tbuffer_load_dwordx4 v[196:199], v27, s[36:39], s58 offen sc1\n"
    "\ts_mov_b64 exec, -1\n"
    "L%=_rp50:\n"
    "\tv_max3_u32 v156, v200, v201, v202\n"
    "\tv_max_u32 v156, v156, v203\n"
    "\tv_cmp_eq_u32 vcc, -1, v156\n"
    "\ts_cbranch_vccz L%=_rp51\n"
    "\ts_or_b64 s[62:63], s[62:63], vcc\n"
    "\ts_mov_b64 exec, vcc\n"
    "\tbuffer_load_dwordx4 v[200:203], v26, s[36:39], s59 offen sc1\n"
    "\ts_mov_b64 exec, -1\n"
    "L%=_rp51:\n"
    "\tv_max3_u32 v156, v204, v205, v206\n"
    "\tv_max_u32 v156, v156, v207\n"
    "\tv_cmp_eq_u32 vcc, -1, v156\n"
    "\ts_cbranch_vccz L%=_rp52\n"
    "\ts_or_b64 s[62:63], s[62:63], vcc\n"
    "\ts_mov_b64 exec, vcc\n"
    "\tbuffer_load_dwordx4 v[204:207], v28, s[36:39], s59 offen sc1\n"
    "\ts_mov_b64 exec, -1\n"
    "L%=_rp52:\n"
    "\ts_add_u32 s73, s73, 1\n"
    "\ts_cmp_lg_u64 s[62:63], 0\n"
    "\ts_cselect_b32 s61, s73, 40000\n"
    "\ts_waitcnt vmcnt(0)\n"
    "\ts_cmp_lt_u32 s61, 40000\n"
    "\ts_cbranch_scc1 L%=_pa20\n"
    "\ts_branch L%=_pd21\n"
    "L%=_pa20:\n"
    "\tv_mfma_f32_16x16x32_bf16 v[160:163], v[48:51], v[176:179], 0\n"
    "\tv_mfma_f32_16x16x32_bf16 v[160:163], v[48:51], v[180:183], v[160:163]\n"
    "\tv_mfma_f32_16x16x32_bf16 v[160:163], v[48:51], v[184:187], v[160:163]\n"
    "\tv_mfma_f32_16x16x32_bf16 v[160:163], v[48:51], v[188:191], v[160:163]\n"
    "\tv_mfma_f32_16x16x32_bf16 v[160:163], v[48:51], v[192:195], v[160:163]\n"
    "\tv_mfma_f32_16x16x32_bf16 v[160:163], v[48:51], v[196:199], v[160:163]\n"
    "\tv_mfma_f32_16x16x32_bf16 v[160:163], v[48:51], v[200:203], v[160:163]\n"
    "\tv_mfma_f32_16x16x32_bf16 v[160:163], v[48:51], v[204:207], v[160:163]\n"
    "\ts_nop 7\n"
    "\tv_cmp_u_f32 vcc, v160, v160\n"
    "\ts_cbranch_vccnz L%=_ps22\n"
    "\ts_branch L%=_pd21\n"
    "L%=_ps26:\n"
    "\ts_mov_b64 s[62:63], 0\n"
    "\tv_max3_u32 v156, v176, v177, v178\n"
    "\tv_max_u32 v156, v156, v179\n"
    "\tv_cmp_eq_u32 vcc, -1, v156\n"
    "\ts_cbranch_vccz L%=_rp53\n"
    "\ts_or_b64 s[62:63], s[62:63], vcc\n"
    "\ts_mov_b64 exec, vcc\n"
    "\tbuffer_load_dwordx4 v[176:179], v26, s[36:39], s56 offen sc1\n"
    "\ts_mov_b64 exec, -1\n"
    "L%=_rp53:\n"
    "\tv_max3_u32 v156, v180, v181, v182\n"
    "\tv_max_u32 v156, v156, v183\n"
    "\tv_cmp_eq_u32 vcc, -1, v156\n"
    "\ts_cbranch_vccz L%=_rp54\n"
    "\ts_or_b64 s[62:63], s[62:63], vcc\n"
    "\ts_mov_b64 exec, vcc\n"
    "\tbuffer_load_dwordx4 v[180:183], v27, s[36:39], s56 offen sc1\n"
    "\ts_mov_b64 exec, -1\n"
    "L%=_rp54:\n"
    "\tv_max3_u32 v156, v184, v185, v186\n"
    "\tv_max_u32 v156, v156, v187\n"
    "\tv_cmp_eq_u32 vcc, -1, v156\n"
    "\ts_cbranch_vccz L%=_rp55\n"
    "\ts_or_b64 s[62:63], s[62:63], vcc\n"
    "\ts_mov_b64 exec, vcc\n"
    "\tbuffer_load_dwordx4 v[184:187], v26, s[36:39], s57 offen sc1\n"
    "\ts_mov_b64 exec, -1\n"
    "L%=_rp55:\n"
    "\tv_max3_u32 v156, v188, v189, v190\n"
    "\tv_max_u32 v156, v156, v191\n"
    "\tv_cmp_eq_u32 vcc, -1, v156\n"
    "\ts_cbranch_vccz L%=_rp56\n"
    "\ts_or_b64 s[62:63], s[62:63], vcc\n"
    "\ts_mov_b64 exec, vcc\n"
    "\tbuffer_load_dwordx4 v[188:191], v27, s[36:39], s57 offen sc1\n"
    "\ts_mov_b64 exec, -1\n"
    "L%=_rp56:\n"
    "\tv_max3_u32 v156, v192, v193, v194\n"
    "\tv_max_u32 v156, v156, v195\n"
    "\tv_cmp_eq_u32 vcc, -1, v156\n"
    "\ts_cbranch_vccz L%=_rp57\n"
    "\ts_or_b64 s[62:63], s[62:63], vcc\n"
    "\ts_mov_b64 exec, vcc\n"
    "\tbuffer_load_dwordx4 v[192:195], v26, s[36:39], s58 offen sc1\n"
    "\ts_mov_b64 exec, -1\n"
    "L%=_rp57:\n"
    "\tv_max3_u32 v156, v196, v197, v198\n"
    "\tv_max_u32 v156, v156, v199\n"
    "\tv_cmp_eq_u32 vcc, -1, v156\n"
    "\ts_cbranch_vccz L%=_rp58\n"
    "\ts_or_b64 s[62:63], s[62:63], vcc\n"
    "\ts_mov_b64 exec, vcc\n"
    "\tbuffer_load_dwordx4 v[196:199], v27, s[36:39], s58 offen sc1\n"
    "\ts_mov_b64 exec, -1\n"
    "L%=_rp58:\n"
    "\tv_max3_u32 v156, v200, v201, v202\n"
    "\tv_max_u32 v156, v156, v203\n"
    "\tv_cmp_eq_u32 vcc, -1, v156\n"
    "\ts_cbranch_vccz L%=_rp59\n"
    "\ts_or_b64 s[62:63], s[62:63], vcc\n"
    "\ts_mov_b64 exec, vcc\n"
    "\tbuffer_load_dwordx4 v[200:203], v26, s[36:39], s59 offen sc1\n"
    "\ts_mov_b64 exec, -1\n"
    "L%=_rp59:\n"
    "\tv_max3_u32 v156, v204, v205, v206\n"
    "\tv_max_u32 v156, v156, v207\n"
    "\tv_cmp_eq_u32 vcc, -1, v156\n"
    "\ts_cbranch_vccz L%=_rp60\n"
    "\ts_or_b64 s[62:63], s[62:63], vcc\n"
    "\ts_mov_b64 exec, vcc\n"
    "\tbuffer_load_dwordx4 v[204:207], v28, s[36:39], s59 offen sc1\n"
    "\ts_mov_b64 exec, -1\n"
    "L%=_rp60:\n"
    "\ts_add_u32 s73, s73, 1\n"
    "\ts_cmp_lg_u64 s[62:63], 0\n"
    "\ts_cselect_b32 s61, s73, 40000\n"
    "\ts_waitcnt vmcnt(0)\n"
    "\ts_cmp_lt_u32 s61, 40000\n"
    "\ts_cbranch_scc1 L%=_pa24\n"
    "\ts_branch L%=_pd25\n"
    "L%=_pa24:\n"
    "\tv_mfma_f32_16x16x32_bf16 v[160:163], v[48:51], v[176:179], 0\n"
    "\tv_mfma_f32_16x16x32_bf16 v[160:163], v[48:51], v[180:183], v[160:163]\n"
    "\tv_mfma_f32_16x16x32_bf16 v[160:163], v[48:51], v[184:187], v[160:163]\n"
    "\tv_mfma_f32_16x16x32_bf16 v[160:163], v[48:51], v[188:191], v[160:163]\n"
    "\tv_mfma_f32_16x16x32_bf16 v[160:163], v[48:51], v[192:195], v[160:163]\n"
    "\tv_mfma_f32_16x16x32_bf16 v[160:163], v[48:51], v[196:199], v[160:163]\n"
    "\tv_mfma_f32_16x16x32_bf16 v[160:163], v[48:51], v[200:203], v[160:163]\n"
    "\tv_mfma_f32_16x16x32_bf16 v[160:163], v[48:51], v[204:207], v[160:163]\n"
    "\ts_nop 7\n"
    "\tv_cmp_u_f32 vcc, v160, v160\n"
    "\ts_cbranch_vccnz L%=_ps26\n"
    "\ts_branch L%=_pd25\n"
    "L%=_ps29:\n"
    "\ts_mov_b64 s[62:63], 0\n"
    "\tv_max3_u32 v156, v176, v177, v178\n"
    "\tv_max_u32 v156, v156, v179\n"
    "\tv_cmp_eq_u32 vcc, -1, v156\n"
    "\ts_cbranch_vccz L%=_rp61\n"
    "\ts_or_b64 s[62:63], s[62:63], vcc\n"
    "\ts_mov_b64 exec, vcc\n"
    "\tbuffer_load_dwordx4 v[176:179], v36, s[36:39], s51 offen offset:0 sc1\n"
    "\ts_mov_b64 exec, -1\n"
    "L%=_rp61:\n"
    "\tv_max3_u32 v156, v180, v181, v182\n"
    "\tv_max_u32 v156, v156, v183\n"
    "\tv_cmp_eq_u32 vcc, -1, v156\n"
    "\ts_cbranch_vccz L%=_rp62\n"
    "\ts_or_b64 s[62:63], s[62:63], vcc\n"
    "\ts_mov_b64 exec, vcc\n"
    "\tbuffer_load_dwordx4 v[180:183], v36, s[36:39], s51 offen offset:1024 sc1\n"
    "\ts_mov_b64 exec, -1\n"
    "L%=_rp62:\n"
    "\tv_max3_u32 v156, v184, v185, v186\n"
    "\tv_max_u32 v156, v156, v187\n"
    "\tv_cmp_eq_u32 vcc, -1, v156\n"
    "\ts_cbranch_vccz L%=_rp63\n"
    "\ts_or_b64 s[62:63], s[62:63], vcc\n"
    "\ts_mov_b64 exec, vcc\n"
    "\tbuffer_load_dwordx4 v[184:187], v36, s[36:39], s51 offen offset:2048 sc1\n"
    "\ts_mov_b64 exec, -1\n"
    "L%=_rp63:\n"
    "\tv_max3_u32 v156, v188, v189, v190\n"
    "\tv_max_u32 v156, v156, v191\n"
    "\tv_cmp_eq_u32 vcc, -1, v156\n"
    "\ts_cbranch_vccz L%=_rp64\n"
    "\ts_or_b64 s[62:63], s[62:63], vcc\n"
    "\ts_mov_b64 exec, vcc\n"
    "\tbuffer_load_dwordx4 v[188:191], v36, s[36:39], s51 offen offset:3072 sc1\n"
    "\ts_mov_b64 exec, -1\n"
    "L%=_rp64:\n"
    "\ts_add_u32 s73, s73, 1\n"
    "\ts_cmp_lg_u64 s[62:63], 0\n"
    "\ts_cselect_b32 s61, s73, 40000\n"
    "\ts_waitcnt vmcnt(0)\n"
    "\ts_cmp_lt_u32 s61, 40000\n"
    "\ts_cbranch_scc1 L%=_pa27\n"
    "\ts_branch L%=_pd28\n"
    "L%=_pa27:\n"
    "\tv_mfma_f32_16x16x32_bf16 v[192:195], v[48:51], v[176:179], 0\n"
    "\tv_mfma_f32_16x16x32_bf16 v[196:199], v[48:51], v[180:183], 0\n"
    "\tv_mfma_f32_16x16x32_bf16 v[200:203], v[48:51], v[184:187], 0\n"
    "\tv_mfma_f32_16x16x32_bf16 v[204:207], v[48:51], v[188:191], 0\n"
    "\ts_nop 5\n"
    "\tv_cmp_u_f32 s[62:63], v192, v196\n"
    "\ts_nop 0\n"
    "\tv_cmp_u_f32 vcc, v200, v204\n"
    "\ts_nop 0\n"
    "\ts_or_b64 vcc, vcc, s[62:63]\n"
    "\ts_cbranch_vccnz L%=_ps29\n"
    "\ts_branch L%=_pd28\n"
    "L%=_ps35:\n"
    "\ts_mov_b64 s[62:63], 0\n"
    "\tv_max3_u32 v156, v176, v177, v178\n"
    "\tv_max_u32 v156, v156, v179\n"
    "\tv_cmp_eq_u32 vcc, -1, v156\n"
    "\ts_cbranch_vccz L%=_rp65\n"
    "\ts_or_b64 s[62:63], s[62:63], vcc\n"
    "\ts_mov_b64 exec, vcc\n"
    "\tbuffer_load_dwordx4 v[176:179], v26, s[36:39], s56 offen sc1\n"
    "\ts_mov_b64 exec, -1\n"
    "L%=_rp65:\n"
    "\tv_max3_u32 v156, v180, v181, v182\n"
    "\tv_max_u32 v156, v156, v183\n"
    "\tv_cmp_eq_u32 vcc, -1, v156\n"
    "\ts_cbranch_vccz L%=_rp66\n"
    "\ts_or_b64 s[62:63], s[62:63], vcc\n"
    "\ts_mov_b64 exec, vcc\n"
    "\tbuffer_load_dwordx4 v[180:183], v27, s[36:39], s56 offen sc1\n"
    "\ts_mov_b64 exec, -1\n"
    "L%=_rp66:\n"
    "\tv_max3_u32 v156, v184, v185, v186\n"
    "\tv_max_u32 v156, v156, v187\n"
    "\tv_cmp_eq_u32 vcc, -1, v156\n"
    "\ts_cbranch_vccz L%=_rp67\n"
    "\ts_or_b64 s[62:63], s[62:63], vcc\n"
    "\ts_mov_b64 exec, vcc\n"
    "\tbuffer_load_dwordx4 v[184:187], v26, s[36:39], s57 offen sc1\n"
    "\ts_mov_b64 exec, -1\n"
    "L%=_rp67:\n"
    "\tv_max3_u32 v156, v188, v189, v190\n"
    "\tv_max_u32 v156, v156, v191\n"
    "\tv_cmp_eq_u32 vcc, -1, v156\n"
    "\ts_cbranch_vccz L%=_rp68\n"
    "\ts_or_b64 s[62:63], s[62:63], vcc\n"
    "\ts_mov_b64 exec, vcc\n"
    "\tbuffer_load_dwordx4 v[188:191], v27, s[36:39], s57 offen sc1\n"
    "\ts_mov_b64 exec, -1\n"
    "L%=_rp68:\n"
    "\ts_add_u32 s73, s73, 1\n"
    "\ts_cmp_lg_u64 s[62:63], 0\n"
    "\ts_cselect_b32 s61, s73, 40000\n"
    "\ts_waitcnt vmcnt(0)\n"
    "\ts_cmp_lt_u32 s61, 40000\n"
    "\ts_cbranch_scc1 L%=_pa33\n"
    "\ts_branch L%=_pd34\n"
    "L%=_pa33:\n"
    "\tv_mfma_f32_16x16x32_bf16 v[160:163], v[48:51], v[176:179], v[52:55]\n"
    "\tv_mfma_f32_16x16x32_bf16 v[160:163], v[48:51], v[180:183], v[160:163]\n"
    "\tv_mfma_f32_16x16x32_bf16 v[160:163], v[48:51], v[184:187], v[160:163]\n"
    "\tv_mfma_f32_16x16x32_bf16 v[160:163], v[48:51], v[188:191], v[160:163]\n"
    "\ts_nop 7\n"
    "\tv_cmp_u_f32 vcc, v160, v160\n"
    "\ts_cbranch_vccnz L%=_ps35\n"
    "\ts_branch L%=_pd34\n"
    "L%=_exit:\n"
    "\ts_endpgm\n"
    "L%=_end:\n"
    :
    : "s"(__builtin_amdgcn_kernarg_segment_ptr()), "s"(blockIdx.x), "v"(threadIdx.x),
      "s"((unsigned)(size_t)(__attribute__((address_space(3))) unsigned char*)lds)
    : "s4", "s5", "s6", "s7", "s8", "s9", "s10", "s11", "s12", "s13", "s14", "s15", "s16", "s17", "s18", "s19", "s20", "s21", "s22", "s23", "s24", "s25", "s26", "s27", "s28", "s29", "s30", "s31", "s33", "s34", "s35", "s36", "s37", "s38", "s39", "s40", "s41", "s42", "s43", "s44", "s45", "s46", "s47", "s48", "s49", "s50", "s51", "s52", "s53", "s54", "s55", "s56", "s57", "s58", "s59", "s60", "s61", "s62", "s63", "s64", "s65", "s66", "s67", "s68", "s69", "s70", "s71", "s72", "s73", "s74", "s75", "s76", "s77", "s78", "s79", "s80", "s81", "s82", "s83", "s84", "s85", "s86", "s87", "s88", "s89", "s90", "s91", "s92", "s93", "s94", "s95", "s96", "s97", "s98", "s99", "v1", "v2", "v3", "v4", "v5", "v6", "v7", "v8", "v9", "v10", "v11", "v12", "v13", "v14", "v15", "v16", "v17", "v18", "v19", "v20", "v21", "v22", "v23", "v24", "v25", "v26", "v27", "v28", "v29", "v30", "v31", "v32", "v33", "v34", "v35", "v36", "v37", "v38", "v39", "v40", "v41", "v42", "v43", "v44", "v45", "v46", "v47", "v48", "v49", "v50", "v51", "v52", "v53", "v54", "v55", "v56", "v57", "v58", "v59", "v60", "v61", "v62", "v63", "v64", "v65", "v66", "v67", "v68", "v69", "v70", "v71", "v72", "v73", "v74", "v75", "v76", "v77", "v78", "v79", "v80", "v81", "v82", "v83", "v84", "v85", "v86", "v87", "v88", "v89", "v90", "v91", "v92", "v93", "v94", "v95", "v96", "v97", "v98", "v99", "v100", "v101", "v102", "v103", "v104", "v105", "v106", "v107", "v108", "v109", "v110", "v111", "v112", "v113", "v114", "v115", "v116", "v117", "v118", "v119", "v120", "v121", "v122", "v123", "v124", "v125", "v126", "v127", "v128", "v129", "v130", "v131", "v132", "v133", "v134", "v135", "v136", "v137", "v138", "v139", "v140", "v141", "v142", "v143", "v144", "v145", "v146", "v147", "v148", "v149", "v150", "v151", "v152", "v153", "v154", "v155", "v156", "v157", "v158", "v159", "v160", "v161", "v162", "v163", "v164", "v165", "v166", "v167", "v168", "v169", "v170", "v171", "v172", "v173", "v174", "v175", "v176", "v177", "v178", "v179", "v180", "v181", "v182", "v183", "v184", "v185", "v186", "v187", "v188", "v189", "v190", "v191", "v192", "v193", "v194", "v195", "v196", "v197", "v198", "v199", "v200", "v201", "v202", "v203", "v204", "v205", "v206", "v207", "v208", "v209", "v210", "v211", "v212", "v213", "v214", "v215", "v216", "v217", "v218", "v219", "v220", "v221", "v222", "v223", "v224", "v225", "v226", "v227", "v228", "v229", "v230", "v231", "v232", "v233", "v234", "v235", "v236", "v237", "v238", "v239", "v240", "v241", "v242", "v243", "v244", "v245", "v246", "v247", "v248", "v249", "v250", "v251", "v252", "v253", "v254", "v255", "vcc", "scc", "memory");
}
}

extern "C" void kernel_launch(void* const* d_in, const int* in_sizes, int n_in, void* d_out, int out_size, void* d_ws,
                              size_t ws_size, hipStream_t stream) {
  Params p;
  p.x = d_in[0]; p.kc = d_in[1]; p.vc = d_in[2]; p.wq = d_in[3]; p.wo = d_in[4]; p.w1 = d_in[6]; p.w2 = d_in[7];
  p.out = d_out;
  p.ws = (unsigned char*)d_ws + WS_OFFSET(ws_size);
  fused_decode<<<256, 512, 0, stream>>>(p);
}
```
